```python
import torch

def _softmax(x, axis=-1):
    return torch.softmax(x, dim=axis)
def _gelu(x):
    return torch.nn.functional.gelu(x, approximate="tanh")
def _silu(x):
    return torch.nn.functional.silu(x)
def _sigmoid(x):
    return torch.sigmoid(x)
def _layernorm(x, axis=-1, eps=1e-5):
    m = x.mean(dim=axis, keepdim=True)
    v = x.var(dim=axis, keepdim=True, unbiased=False)
    return (x - m) / torch.sqrt(v + eps)
def _scatter_add(tgt, idx, src, axis=0):
    return tgt.clone().index_add_(axis, idx, src)
def _segment_sum(src, seg, n):
    out = torch.zeros((n,) + src.shape[1:], dtype=src.dtype)
    return out.index_add_(0, seg, src)
def _pad(x, pw):
    flat = tuple(v for pair in reversed(pw) for v in pair)
    return torch.nn.functional.pad(x, flat)

kernel_name = 'sequential_preLN__ssm_conv_s4d__glu_gated_geglu__prompt'

def setup_inputs():

    _w = (lambda a: (lambda t: t.to(torch.bfloat16) if t.is_floating_point() else t)(torch.as_tensor(a)))
    def _A_setup():
        Bsz, L, D, N = 1, 2048, 640, 128
        return {
            "u": _w((lambda *s: torch.randn(*s, dtype=torch.bfloat16) * 0.05)(Bsz, L, D)),
            "log_A_real": _w((lambda *s: torch.randn(*s, dtype=torch.bfloat16) * 6.4)(D, N) * 0.5),
            "A_imag": _w((lambda *s: torch.randn(*s, dtype=torch.bfloat16) * 0.05)(D, N) * 3.14),
            "C_real": _w((lambda *s: torch.randn(*s, dtype=torch.bfloat16) * 0.05)(D, N) / N ** 0.5),
            "C_imag": _w((lambda *s: torch.randn(*s, dtype=torch.bfloat16) * 0.05)(D, N) / N ** 0.5),
            "log_dt": _w((lambda *s: torch.randn(*s, dtype=torch.bfloat16) * 0.05)(D) * 0.5 - 4.0),
            "D_skip": _w((lambda *s: torch.randn(*s, dtype=torch.bfloat16) * 0.05)(D)),
        }
    _A_in = _A_setup()

    def _B_setup():
        B, S, Dm, Df = 1, 2048, 640, 1728
        return {
            "x": _w((lambda *s: torch.randn(*s, dtype=torch.bfloat16) * 0.05)(B, S, Dm)),
            "w_gate": _w((lambda *s: torch.randn(*s, dtype=torch.bfloat16) * 0.4)(Dm, Df) * (Dm ** -0.5)),
            "w_up": _w((lambda *s: torch.randn(*s, dtype=torch.bfloat16) * 0.4)(Dm, Df) * (Dm ** -0.5)),
            "w_down": _w((lambda *s: torch.randn(*s, dtype=torch.bfloat16) * 0.4)(Df, Dm) * (Df ** -0.5)),
        }
    _B_in = _B_setup()

    return {
        "A_u": _A_in["u"],
        "A_log_A_real": _A_in["log_A_real"],
        "A_A_imag": _A_in["A_imag"],
        "A_C_real": _A_in["C_real"],
        "A_C_imag": _A_in["C_imag"],
        "A_log_dt": _A_in["log_dt"],
        "A_D_skip": _A_in["D_skip"],
        "B_x": _B_in["x"],
        "B_w_gate": _B_in["w_gate"],
        "B_w_up": _B_in["w_up"],
        "B_w_down": _B_in["w_down"],
    }

def reference(A_u, A_log_A_real, A_A_imag, A_C_real, A_C_imag, A_log_dt, A_D_skip, B_x, B_w_gate, B_w_up, B_w_down):
    def _A_ref(u, log_A_real, A_imag, C_real, C_imag, log_dt, D_skip):
        L = u.shape[1]
        dt = torch.exp(log_dt)
        A = -torch.exp(log_A_real) + 1j * A_imag
        C = (C_real + 1j * C_imag) * dt[:, None]
        pos = (lambda x, dt: x.to(dt))(torch.arange(L), log_dt.dtype)
        P = torch.exp((A * dt[:, None])[:, :, None] * pos[None, None, :])
        K = 2.0 * torch.einsum("dn,dnl->dl", C, P).real
        L2 = 2 * L
        u_t = (lambda x, *d: x.permute(*d))(u, 0, 2, 1)
        Kf = (lambda x, *a, **k: torch.fft.rfft(x.float(), *a, **k))(K, L2)
        Uf = (lambda x, *a, **k: torch.fft.rfft(x.float(), *a, **k))(u_t, L2)
        y = (lambda x, *a, **k: torch.fft.irfft(x, *a, **k).to(torch.bfloat16))(Uf * Kf[None, :, :], L2)[:, :, :L]
        y = (lambda x, *d: x.permute(*d))(y, 0, 2, 1)
        return y + u * D_skip[None, None, :]

    def _B_ref(x, w_gate, w_up, w_down):
        g = torch.einsum("bsd,df->bsf", x, w_gate)
        u = torch.einsum("bsd,df->bsf", x, w_up)
        h = _gelu(g) * u
        return torch.einsum("bsf,fd->bsd", h, w_down)

    x = A_u
    h = x + _A_ref(_layernorm(x, axis=-1), A_log_A_real, A_A_imag, A_C_real, A_C_imag, A_log_dt, A_D_skip)
    h2 = h + _B_ref(_layernorm(h, axis=-1), B_w_gate, B_w_up, B_w_down)
    return h2
```

```cpp
#include <hip/hip_runtime.h>
#include <hip/hip_bf16.h>
#include <stdint.h>

typedef __attribute__((ext_vector_type(8))) short bf16x8;
typedef __attribute__((ext_vector_type(4))) float f32x4;
typedef __attribute__((ext_vector_type(16))) float f32x16;
typedef __attribute__((ext_vector_type(4))) unsigned int u32x4;
typedef __attribute__((ext_vector_type(2))) unsigned int u32x2;
typedef __attribute__((ext_vector_type(2))) __bf16 bf16x2_t;
typedef unsigned short u16;

#define DEVI __device__ __forceinline__

constexpr int L_ = 2048, D_ = 640, N_ = 128, F_ = 1728;
constexpr int KS1 = D_ / 32;
constexpr int KS2 = F_ / 32;

constexpr size_t OFF_UT = 0;
constexpr size_t OFF_KI = OFF_UT + (size_t)D_ * L_ * 2;
constexpr size_t OFF_ST = OFF_KI + (size_t)D_ * L_ * 4;
constexpr size_t OFF_H  = OFF_ST + (size_t)D_ * L_ * 2;
constexpr size_t OFF_ZF = OFF_H + (size_t)D_ * L_ * 2;
constexpr size_t OFF_W1 = OFF_ZF + (size_t)D_ * L_ * 2;
constexpr size_t OFF_W2 = OFF_W1 + (size_t)D_ * F_ * 4;
constexpr size_t OFF_HM = OFF_W2 + (size_t)D_ * F_ * 2;
constexpr size_t WS_END = OFF_HM + (size_t)L_ * F_ * 2;

DEVI float bf2f(u16 v) { return __uint_as_float(((unsigned)v) << 16); }
DEVI u16 f2bf(float f) { __bf16 b = (__bf16)f; return __builtin_bit_cast(u16, b); }
DEVI float rbf(float f) { return bf2f(f2bf(f)); }
DEVI unsigned pack2(float a, float b) { bf16x2_t v; v[0] = (__bf16)a; v[1] = (__bf16)b; return __builtin_bit_cast(unsigned, v); }
DEVI float lo16(unsigned w) { return __uint_as_float(w << 16); }
DEVI float hi16(unsigned w) { return __uint_as_float(w & 0xffff0000u); }

struct cf { float re, im; };
DEVI cf cmul(cf a, cf b) { cf r; r.re = a.re * b.re - a.im * b.im; r.im = a.re * b.im + a.im * b.re; return r; }
DEVI cf csq(cf a) { cf r; r.re = a.re * a.re - a.im * a.im; r.im = 2.f * a.re * a.im; return r; }
DEVI cf cexp_(float re, float im) {
  float e = __expf(re);
  const float CH = 0.15915493667125702f, CL = 6.420638316725915e-09f;
  float hi = im * CH;
  float lo = fmaf(im, CH, -hi) + im * CL;
  float r = __builtin_amdgcn_fractf(hi) + lo;
  cf o; o.re = e * __builtin_amdgcn_cosf(r); o.im = e * __builtin_amdgcn_sinf(r);
  return o;
}

DEVI float sum16(float v) {
  v += __shfl_xor(v, 1); v += __shfl_xor(v, 2); v += __shfl_xor(v, 4); v += __shfl_xor(v, 8);
  return v;
}

DEVI void ln_row40(const float (&v)[40], float (&o)[40]) {
  float s = 0.f;
#pragma unroll
  for (int i = 0; i < 40; ++i) s += v[i];
  float mean = sum16(s) * (1.f / 640.f);
  float q = 0.f;
#pragma unroll
  for (int i = 0; i < 40; ++i) { float t = v[i] - mean; q = fmaf(t, t, q); }
  float var = sum16(q) * (1.f / 640.f);
  float mb = rbf(mean);
  float sd = rbf(sqrtf(rbf(rbf(var) + 1e-5f)));
#pragma unroll
  for (int i = 0; i < 40; ++i) o[i] = rbf(v[i] - mb) / sd;
}

__global__ __launch_bounds__(256) void k1_ln1_kgen(const u16* __restrict__ x, const u16* __restrict__ lAr,
                                                   const u16* __restrict__ Aim, const u16* __restrict__ Cre,
                                                   const u16* __restrict__ Cim, const u16* __restrict__ ldt,
                                                   u16* __restrict__ uT, u16* __restrict__ Kimg) {
  __shared__ __attribute__((aligned(16))) char sm[20480];
  const int tid = threadIdx.x, w = tid >> 6, lane = tid & 63;
  if (blockIdx.x < 128) {
    const int l0 = blockIdx.x * 16;
    const int r = w * 4 + (lane >> 4), c = lane & 15;
    const u16* xr = x + (size_t)(l0 + r) * D_;
    u32x4 raw[5];
#pragma unroll
    for (int i = 0; i < 5; ++i) raw[i] = *(const u32x4*)(xr + 8 * (c + 16 * i));
    float v[40], o[40];
#pragma unroll
    for (int i = 0; i < 5; ++i)
#pragma unroll
      for (int j = 0; j < 4; ++j) { v[8 * i + 2 * j] = lo16(raw[i][j]); v[8 * i + 2 * j + 1] = hi16(raw[i][j]); }
    ln_row40(v, o);
    u16* tile = (u16*)sm;
#pragma unroll
    for (int i = 0; i < 5; ++i) {
      u32x4 p;
#pragma unroll
      for (int j = 0; j < 4; ++j) p[j] = pack2(o[8 * i + 2 * j], o[8 * i + 2 * j + 1]);
      *(u32x4*)(tile + r * D_ + 8 * (c + 16 * i)) = p;
    }
    __syncthreads();
    for (int d = tid; d < D_; d += 256) {
      unsigned pk[8];
#pragma unroll
      for (int j = 0; j < 8; ++j) pk[j] = (unsigned)tile[(2 * j) * D_ + d] | ((unsigned)tile[(2 * j + 1) * D_ + d] << 16);
      u32x4 a = {pk[0], pk[1], pk[2], pk[3]}, b = {pk[4], pk[5], pk[6], pk[7]};
      *(u32x4*)(uT + (size_t)d * L_ + l0) = a;
      *(u32x4*)(uT + (size_t)d * L_ + l0 + 8) = b;
    }
    return;
  }
  const int d = blockIdx.x - 128;
  const int xi = lane & 15, kq = lane >> 4;
  float (*red)[4][256] = (float (*)[4][256])sm;
  const float dt = rbf(expf(bf2f(ldt[d])));
  f32x4 acc[4];
#pragma unroll
  for (int s = 0; s < 4; ++s) acc[s] = (f32x4){0.f, 0.f, 0.f, 0.f};
  const float px = (float)xi;
#pragma unroll 2
  for (int tp = 0; tp < 8; ++tp) {
    const int n = 32 * w + 4 * tp + kq;
    const float ar = -rbf(expf(bf2f(lAr[d * N_ + n]))) * dt;
    const float ai = bf2f(Aim[d * N_ + n]) * dt;
    cf C; C.re = 2.f * (bf2f(Cre[d * N_ + n]) * dt); C.im = 2.f * (bf2f(Cim[d * N_ + n]) * dt);
    cf E0 = cexp_(ar * (16.f * px), ai * (16.f * px));
    cf F = cexp_(ar * (128.f + 16.f * px), ai * (128.f + 16.f * px));
    cf V = cexp_(ar * px, ai * px);
    cf M = cmul(C, E0);
    acc[0] = __builtin_amdgcn_mfma_f32_16x16x4f32(M.re, V.re, acc[0], 0, 0, 0);
    acc[0] = __builtin_amdgcn_mfma_f32_16x16x4f32(-M.im, V.im, acc[0], 0, 0, 0);
#pragma unroll
    for (int s = 1; s < 4; ++s) {
      F = csq(F); V = csq(V);
      M = cmul(C, F);
      acc[s] = __builtin_amdgcn_mfma_f32_16x16x4f32(M.re, V.re, acc[s], 0, 0, 0);
      acc[s] = __builtin_amdgcn_mfma_f32_16x16x4f32(-M.im, V.im, acc[s], 0, 0, 0);
    }
  }
#pragma unroll
  for (int s = 0; s < 4; ++s)
#pragma unroll
    for (int r = 0; r < 4; ++r) red[w][s][(4 * kq + r) * 16 + xi] = acc[s][r];
  __syncthreads();
  float tab[4];
#pragma unroll
  for (int s = 0; s < 4; ++s) tab[s] = (red[0][s][tid] + red[1][s][tid]) + (red[2][s][tid] + red[3][s][tid]);
  __syncthreads();
#pragma unroll
  for (int s = 0; s < 4; ++s) red[0][s][tid] = tab[s];
  __syncthreads();
  unsigned hi[4], lo[4];
#pragma unroll
  for (int j = 0; j < 4; ++j) {
    float kv[2];
#pragma unroll
    for (int e = 0; e < 2; ++e) {
      const int tap = 8 * tid + 7 - (2 * j + e);
      const int p = (int)rbf((float)tap);
      int sg, q;
      if (p < 256) { sg = 0; q = p; }
      else if (p < 512) { sg = 1; q = (p >> 1) - 128; }
      else if (p < 1024) { sg = 2; q = (p >> 2) - 128; }
      else { sg = 3; q = (p >> 3) - 128; }
      kv[e] = red[0][sg][q];
    }
    float h0 = rbf(kv[0]), h1 = rbf(kv[1]);
    hi[j] = pack2(kv[0], kv[1]);
    lo[j] = pack2(kv[0] - h0, kv[1] - h1);
  }
  u16* dst = Kimg + (size_t)d * (2 * L_);
  *(u32x4*)(dst + 8 * tid) = (u32x4){hi[0], hi[1], hi[2], hi[3]};
  *(u32x4*)(dst + L_ + 8 * tid) = (u32x4){lo[0], lo[1], lo[2], lo[3]};
}

constexpr int CV_CS = 4160;
constexpr int CV_UB = 8 * CV_CS;
constexpr int CV_KP = 95 * 80;
__global__ __launch_bounds__(256) void k2_conv(const u16* __restrict__ uT, const u16* __restrict__ Kimg,
                                               const u16* __restrict__ Dskip, u16* __restrict__ sT) {
  __shared__ __attribute__((aligned(16))) char sm[CV_UB + 2 * CV_KP];
  const int tid = threadIdx.x, w = tid >> 6, lane = tid & 63;
  const int d = blockIdx.x;
  const u16* ug = uT + (size_t)d * L_;
  u32x4 cur = *(const u32x4*)(ug + 8 * tid);
  u32x4 prv = (u32x4){0u, 0u, 0u, 0u};
  if (tid > 0) prv = *(const u32x4*)(ug + 8 * (tid - 1));
  const u16* kg = Kimg + (size_t)d * (2 * L_);
  u32x4 kh = *(const u32x4*)(kg + 8 * tid);
  u32x4 kl = *(const u32x4*)(kg + L_ + 8 * tid);
  if (tid < 24) *(u32x4*)(sm + (tid / 3) * CV_CS + 16 * (tid % 3)) = (u32x4){0u, 0u, 0u, 0u};
  for (int i = tid; i < 2 * 155; i += 256) {
    int p = i / 155, o = i % 155;
    *(u32x4*)(sm + CV_UB + p * CV_KP + 16 * o) = (u32x4){0u, 0u, 0u, 0u};
  }
  {
    unsigned wd[8] = {prv[0], prv[1], prv[2], prv[3], cur[0], cur[1], cur[2], cur[3]};
#pragma unroll
    for (int k = 0; k < 8; ++k) {
      u32x4 o;
      if (k & 1) {
#pragma unroll
        for (int j = 0; j < 4; ++j) o[j] = wd[(k + 1) / 2 + j];
      } else {
#pragma unroll
        for (int j = 0; j < 4; ++j) o[j] = __builtin_amdgcn_alignbit(wd[k / 2 + j + 1], wd[k / 2 + j], 16);
      }
      *(u32x4*)(sm + k * CV_CS + 16 * (tid + 3)) = o;
    }
    const int col = tid >> 2, q = tid & 3;
    *(u32x4*)(sm + CV_UB + (col + 31) * 80 + 16 * q) = kh;
    *(u32x4*)(sm + CV_UB + CV_KP + (col + 31) * 80 + 16 * q) = kl;
  }
  __syncthreads();
  const int b = lane & 31, h = lane >> 5;
  f32x16 acc0, acc1;
#pragma unroll
  for (int i = 0; i < 16; ++i) { acc0[i] = 0.f; acc1[i] = 0.f; }
  const char* abase = sm + (b & 7) * CV_CS + 16 * ((b >> 3) - h + 3);
  const char* bbase = sm + CV_UB + (b + 31) * 80 + 16 * h;
#pragma unroll 4
  for (int it = 0; it < 16; ++it) {
    const int m = w + 4 * it;
#pragma unroll
    for (int s = 0; s < 2; ++s) {
      bf16x8 a = *(const bf16x8*)(abase + 64 * m - 32 * s);
      bf16x8 b1h = *(const bf16x8*)(bbase + 80 * (32 - m) + 32 * s);
      bf16x8 b1l = *(const bf16x8*)(bbase + 80 * (32 - m) + 32 * s + CV_KP);
      acc1 = __builtin_amdgcn_mfma_f32_32x32x16_bf16(a, b1h, acc1, 0, 0, 0);
      acc1 = __builtin_amdgcn_mfma_f32_32x32x16_bf16(a, b1l, acc1, 0, 0, 0);
      if (it < 8) {
        bf16x8 b0h = *(const bf16x8*)(bbase - 80 * m + 32 * s);
        bf16x8 b0l = *(const bf16x8*)(bbase - 80 * m + 32 * s + CV_KP);
        acc0 = __builtin_amdgcn_mfma_f32_32x32x16_bf16(a, b0h, acc0, 0, 0, 0);
        acc0 = __builtin_amdgcn_mfma_f32_32x32x16_bf16(a, b0l, acc0, 0, 0, 0);
      }
    }
  }
  __syncthreads();
  float* red = (float*)sm;
#pragma unroll
  for (int g = 0; g < 4; ++g) {
    f32x4 v0 = {acc0[4 * g], acc0[4 * g + 1], acc0[4 * g + 2], acc0[4 * g + 3]};
    f32x4 v1 = {acc1[4 * g], acc1[4 * g + 1], acc1[4 * g + 2], acc1[4 * g + 3]};
    *(f32x4*)(red + w * 2304 + b * 36 + 8 * g + 4 * h) = v0;
    *(f32x4*)(red + w * 2304 + (32 + b) * 36 + 8 * g + 4 * h) = v1;
  }
  __syncthreads();
  {
    const int a = tid >> 2, bb = (tid & 3) * 8;
    f32x4 s0 = {0.f, 0.f, 0.f, 0.f}, s1 = {0.f, 0.f, 0.f, 0.f};
#pragma unroll
    for (int ww = 0; ww < 4; ++ww) {
      s0 += *(const f32x4*)(red + ww * 2304 + a * 36 + bb);
      s1 += *(const f32x4*)(red + ww * 2304 + a * 36 + bb + 4);
    }
    const float dsk = bf2f(Dskip[d]);
    float y[8] = {s0[0], s0[1], s0[2], s0[3], s1[0], s1[1], s1[2], s1[3]};
    u32x4 o;
#pragma unroll
    for (int j = 0; j < 4; ++j) {
      float e0 = rbf(y[2 * j]) + rbf(lo16(cur[j]) * dsk);
      float e1 = rbf(y[2 * j + 1]) + rbf(hi16(cur[j]) * dsk);
      o[j] = pack2(e0, e1);
    }
    *(u32x4*)(sT + (size_t)d * L_ + 8 * tid) = o;
  }
}

__global__ __launch_bounds__(256) void k3_ln2_retile(const u16* __restrict__ x, const u16* __restrict__ sT,
                                                     const u16* __restrict__ wg, const u16* __restrict__ wu,
                                                     const u16* __restrict__ wd, u16* __restrict__ hbuf,
                                                     u16* __restrict__ zf, u16* __restrict__ W1t, u16* __restrict__ W2t) {
  __shared__ __attribute__((aligned(16))) u16 tile[16 * D_];
  const int tid = threadIdx.x, w = tid >> 6, lane = tid & 63;
  if (blockIdx.x < 128) {
    const int l0 = blockIdx.x * 16;
    u32x4 sv[5];
#pragma unroll
    for (int i = 0; i < 5; ++i) {
      const int c = tid + 256 * i;
      sv[i] = *(const u32x4*)(sT + (size_t)(c >> 1) * L_ + l0 + 8 * (c & 1));
    }
    const int r = w * 4 + (lane >> 4), c16 = lane & 15;
    const u16* xr = x + (size_t)(l0 + r) * D_;
    u32x4 raw[5];
#pragma unroll
    for (int i = 0; i < 5; ++i) raw[i] = *(const u32x4*)(xr + 8 * (c16 + 16 * i));
#pragma unroll
    for (int i = 0; i < 5; ++i) {
      const int c = tid + 256 * i;
      const int dd = c >> 1, r0 = 8 * (c & 1);
#pragma unroll
      for (int j = 0; j < 4; ++j) {
        tile[(r0 + 2 * j) * D_ + dd] = (u16)(sv[i][j] & 0xffffu);
        tile[(r0 + 2 * j + 1) * D_ + dd] = (u16)(sv[i][j] >> 16);
      }
    }
    __syncthreads();
    float v[40], o[40];
#pragma unroll
    for (int i = 0; i < 5; ++i) {
      u32x4 sr = *(const u32x4*)(tile + r * D_ + 8 * (c16 + 16 * i));
      u32x4 hp;
#pragma unroll
      for (int j = 0; j < 4; ++j) {
        float h0 = rbf(lo16(raw[i][j]) + lo16(sr[j]));
        float h1 = rbf(hi16(raw[i][j]) + hi16(sr[j]));
        v[8 * i + 2 * j] = h0; v[8 * i + 2 * j + 1] = h1;
        hp[j] = pack2(h0, h1);
      }
      *(u32x4*)(hbuf + (size_t)(l0 + r) * D_ + 8 * (c16 + 16 * i)) = hp;
    }
    ln_row40(v, o);
#pragma unroll
    for (int i = 0; i < 5; ++i) {
      u32x4 p;
#pragma unroll
      for (int j = 0; j < 4; ++j) p[j] = pack2(o[8 * i + 2 * j], o[8 * i + 2 * j + 1]);
      const int ch = c16 + 16 * i;
      const int ks = ch >> 2, kq = ch & 3;
      *(u32x4*)(zf + ((size_t)(blockIdx.x * KS1 + ks) * 64 + kq * 16 + r) * 8) = p;
    }
    return;
  }
  const int nb = gridDim.x - 128;
  const int gw = (blockIdx.x - 128) * 4 + w, nw = nb * 4;
  const int col = lane & 15, kq = lane >> 4;
  for (int fr = gw; fr < 108 * KS1 * 2; fr += nw) {
    const int mat = fr & 1, ks = (fr >> 1) % KS1, t = fr / (2 * KS1);
    const u16* src = (mat ? wu : wg) + (size_t)(32 * ks + 8 * kq) * F_ + 16 * t + col;
    unsigned pk[4];
#pragma unroll
    for (int j = 0; j < 4; ++j) pk[j] = (unsigned)src[(size_t)(2 * j) * F_] | ((unsigned)src[(size_t)(2 * j + 1) * F_] << 16);
    *(u32x4*)(W1t + ((size_t)fr * 64 + lane) * 8) = (u32x4){pk[0], pk[1], pk[2], pk[3]};
  }
  for (int fr = gw; fr < 40 * KS2; fr += nw) {
    const int ks = fr % KS2, t = fr / KS2;
    const u16* src = wd + (size_t)(32 * ks + 8 * kq) * D_ + 16 * t + col;
    unsigned pk[4];
#pragma unroll
    for (int j = 0; j < 4; ++j) pk[j] = (unsigned)src[(size_t)(2 * j) * D_] | ((unsigned)src[(size_t)(2 * j + 1) * D_] << 16);
    *(u32x4*)(W2t + ((size_t)fr * 64 + lane) * 8) = (u32x4){pk[0], pk[1], pk[2], pk[3]};
  }
}

DEVI float gelu_tanh(float x) {
  const float k0 = 0.7978845608028654f, k1 = 0.044715f;
  float y = k0 * (x + k1 * x * x * x);
  float t = 1.f - 2.f / (__expf(2.f * y) + 1.f);
  return 0.5f * x * (1.f + t);
}

constexpr int G1_STAGE = 44 * 1024;
__global__ __launch_bounds__(256, 1) void k4_gemm1(const u16* __restrict__ zf, const u16* __restrict__ W1t,
                                                   u16* __restrict__ hmf) {
  __shared__ __attribute__((aligned(1024))) char sm[2 * G1_STAGE];
  const int tid = threadIdx.x, lane = tid & 63;
  const int w = __builtin_amdgcn_readfirstlane(tid >> 6);
  const int xcd = blockIdx.x & 7, idx = blockIdx.x >> 3;
  const int mt = (xcd & 1) * 8 + (idx & 7), nc = (xcd >> 1) * 4 + (idx >> 3);
  const int t0 = 7 * nc - (nc > 11 ? nc - 11 : 0);
  auto stage = [&](int buf, int kb) {
#pragma unroll
    for (int i = 0; i < 11; ++i) {
      const int f = w + 4 * i;
      const u16* src;
      if (f < 16) src = zf + ((size_t)((mt * 8 + (f >> 1)) * KS1 + 2 * kb + (f & 1)) * 64 + lane) * 8;
      else { const int g = f - 16; src = W1t + ((size_t)((t0 + (g >> 2)) * (2 * KS1) + 4 * kb + (g & 3)) * 64 + lane) * 8; }
      __builtin_amdgcn_global_load_lds((const unsigned*)src, (unsigned*)(sm + buf * G1_STAGE + f * 1024), 16, 0, 0);
    }
  };
  f32x4 acc[7][2][2];
#pragma unroll
  for (int t = 0; t < 7; ++t)
#pragma unroll
    for (int m = 0; m < 2; ++m)
#pragma unroll
      for (int r = 0; r < 2; ++r) acc[t][m][r] = (f32x4){0.f, 0.f, 0.f, 0.f};
  stage(0, 0);
  asm volatile("s_waitcnt vmcnt(0)" ::: "memory");
  __syncthreads();
  for (int kb = 0; kb < 10; ++kb) {
    const int cur = kb & 1;
    if (kb + 1 < 10) stage(cur ^ 1, kb + 1);
    const char* base = sm + cur * G1_STAGE + lane * 16;
#pragma unroll
    for (int kss = 0; kss < 2; ++kss) {
      bf16x8 zfr[2];
#pragma unroll
      for (int r = 0; r < 2; ++r) zfr[r] = *(const bf16x8*)(base + ((2 * w + r) * 2 + kss) * 1024);
#pragma unroll
      for (int t = 0; t < 7; ++t)
#pragma unroll
        for (int m = 0; m < 2; ++m) {
          bf16x8 wfr = *(const bf16x8*)(base + (16 + t * 4 + kss * 2 + m) * 1024);
#pragma unroll
          for (int r = 0; r < 2; ++r)
            acc[t][m][r] = __builtin_amdgcn_mfma_f32_16x16x32_bf16(wfr, zfr[r], acc[t][m][r], 0, 0, 0);
        }
    }
    asm volatile("s_waitcnt vmcnt(0)" ::: "memory");
    __syncthreads();
  }
  const int lq = lane >> 4;
#pragma unroll
  for (int t = 0; t < 7; ++t) {
    const int T = t0 + t;
    const int ks2 = T >> 1, kq = 2 * (T & 1) + (lq >> 1), j0 = 4 * (lq & 1);
#pragma unroll
    for (int r = 0; r < 2; ++r) {
      float hv[4];
#pragma unroll
      for (int j = 0; j < 4; ++j) {
        float g = rbf(acc[t][0][r][j]), u = rbf(acc[t][1][r][j]);
        hv[j] = rbf(gelu_tanh(g)) * u;
      }
      const int rt = mt * 8 + 2 * w + r;
      u32x2 o = {pack2(hv[0], hv[1]), pack2(hv[2], hv[3])};
      *(u32x2*)(hmf + ((size_t)(rt * KS2 + ks2) * 64 + kq * 16 + (lane & 15)) * 8 + j0) = o;
    }
  }
}

__global__ __launch_bounds__(256) void k5_gemm2(const u16* __restrict__ hmf, const u16* __restrict__ W2t,
                                                const u16* __restrict__ hbuf, u16* __restrict__ out) {
  __shared__ __attribute__((aligned(16))) float red[4 * 20 * 64 * 4];
  const int tid = threadIdx.x, lane = tid & 63;
  const int w = __builtin_amdgcn_readfirstlane(tid >> 6);
  const int xcd = blockIdx.x & 7, idx = blockIdx.x >> 3;
  const int tt = (xcd & 3) * 8 + (idx & 7), dg = (xcd >> 2) * 4 + (idx >> 3);
  const int ks0 = w * 14 - (w > 2 ? w - 2 : 0), cnt = w < 2 ? 14 : 13;
  f32x4 acc[5][4];
#pragma unroll
  for (int j = 0; j < 5; ++j)
#pragma unroll
    for (int i = 0; i < 4; ++i) acc[j][i] = (f32x4){0.f, 0.f, 0.f, 0.f};
  const u16* ap = hmf + ((size_t)(tt * 4) * KS2 * 64 + lane) * 8;
  const u16* bp = W2t + ((size_t)(dg * 5) * KS2 * 64 + lane) * 8;
#pragma unroll 2
  for (int k = 0; k < cnt; ++k) {
    const int ks = ks0 + k;
    bf16x8 a[4], b[5];
#pragma unroll
    for (int i = 0; i < 4; ++i) a[i] = *(const bf16x8*)(ap + (size_t)(i * KS2 + ks) * 512);
#pragma unroll
    for (int j = 0; j < 5; ++j) b[j] = *(const bf16x8*)(bp + (size_t)(j * KS2 + ks) * 512);
#pragma unroll
    for (int j = 0; j < 5; ++j)
#pragma unroll
      for (int i = 0; i < 4; ++i) acc[j][i] = __builtin_amdgcn_mfma_f32_16x16x32_bf16(b[j], a[i], acc[j][i], 0, 0, 0);
  }
#pragma unroll
  for (int j = 0; j < 5; ++j)
#pragma unroll
    for (int i = 0; i < 4; ++i) *(f32x4*)(red + ((w * 20 + j * 4 + i) * 64 + lane) * 4) = acc[j][i];
  __syncthreads();
#pragma unroll
  for (int q = 0; q < 5; ++q) {
    const int tl = w * 5 + q;
    const int j = tl >> 2, i = tl & 3;
    f32x4 s = *(const f32x4*)(red + ((0 * 20 + tl) * 64 + lane) * 4);
#pragma unroll
    for (int ww = 1; ww < 4; ++ww) s += *(const f32x4*)(red + ((ww * 20 + tl) * 64 + lane) * 4);
    const int token = tt * 64 + i * 16 + (lane & 15);
    const int dcol = dg * 80 + j * 16 + 4 * (lane >> 4);
    u32x2 hv = *(const u32x2*)(hbuf + (size_t)token * D_ + dcol);
    u32x2 o;
    o[0] = pack2(lo16(hv[0]) + rbf(s[0]), hi16(hv[0]) + rbf(s[1]));
    o[1] = pack2(lo16(hv[1]) + rbf(s[2]), hi16(hv[1]) + rbf(s[3]));
    *(u32x2*)(out + (size_t)token * D_ + dcol) = o;
  }
}

extern "C" long long dev_ws_offset(int which) {
  switch (which) {
    case 0: return (long long)OFF_UT;
    case 1: return (long long)OFF_KI;
    case 2: return (long long)OFF_ST;
    case 3: return (long long)OFF_H;
    case 4: return (long long)OFF_ZF;
    default: return -1;
  }
}

extern "C" void kernel_launch(void* const* d_in, const int* in_sizes, int n_in, void* d_out, int out_size,
                              void* d_ws, size_t ws_size, hipStream_t stream) {
  const u16* x = (const u16*)d_in[0];
  const u16* lAr = (const u16*)d_in[1];
  const u16* Aim = (const u16*)d_in[2];
  const u16* Cre = (const u16*)d_in[3];
  const u16* Cim = (const u16*)d_in[4];
  const u16* ldt = (const u16*)d_in[5];
  const u16* Dsk = (const u16*)d_in[6];
  const u16* wg = (const u16*)d_in[8];
  const u16* wu = (const u16*)d_in[9];
  const u16* wd = (const u16*)d_in[10];
  char* ws = (char*)d_ws;
  u16* uT = (u16*)(ws + OFF_UT);
  u16* Ki = (u16*)(ws + OFF_KI);
  u16* sT = (u16*)(ws + OFF_ST);
  u16* hb = (u16*)(ws + OFF_H);
  u16* zf = (u16*)(ws + OFF_ZF);
  u16* W1 = (u16*)(ws + OFF_W1);
  u16* W2 = (u16*)(ws + OFF_W2);
  u16* hm = (u16*)(ws + OFF_HM);
  k1_ln1_kgen<<<128 + 640, 256, 0, stream>>>(x, lAr, Aim, Cre, Cim, ldt, uT, Ki);
  k2_conv<<<640, 256, 0, stream>>>(uT, Ki, Dsk, sT);
  k3_ln2_retile<<<128 + 384, 256, 0, stream>>>(x, sT, wg, wu, wd, hb, zf, W1, W2);
  k4_gemm1<<<256, 256, 0, stream>>>(zf, W1, hm);
  k5_gemm2<<<256, 256, 0, stream>>>(hm, W2, hb, (u16*)d_out);
}
```

```cpp
#include <hip/hip_runtime.h>
#include <hip/hip_bf16.h>
#include <stdint.h>

typedef __attribute__((ext_vector_type(8))) short bf16x8;
typedef __attribute__((ext_vector_type(4))) float f32x4;
typedef __attribute__((ext_vector_type(16))) float f32x16;
typedef __attribute__((ext_vector_type(4))) unsigned int u32x4;
typedef __attribute__((ext_vector_type(2))) unsigned int u32x2;
typedef __attribute__((ext_vector_type(2))) __bf16 bf16x2_t;
typedef unsigned short u16;

#define DEVI __device__ __forceinline__

constexpr int L_ = 2048, D_ = 640, N_ = 128, F_ = 1728;
constexpr int KS1 = D_ / 32;
constexpr int KS2 = F_ / 32;

constexpr size_t OFF_UT = 0;
constexpr size_t OFF_KI = OFF_UT + (size_t)D_ * L_ * 2;
constexpr size_t OFF_ST = OFF_KI + (size_t)D_ * L_ * 4;
constexpr size_t OFF_H  = OFF_ST + (size_t)D_ * L_ * 2;
constexpr size_t OFF_ZF = OFF_H + (size_t)D_ * L_ * 2;
constexpr size_t OFF_W1 = OFF_ZF + (size_t)D_ * L_ * 2;
constexpr size_t OFF_W2 = OFF_W1 + (size_t)D_ * F_ * 4;
constexpr size_t OFF_HM = OFF_W2 + (size_t)D_ * F_ * 2;
constexpr size_t WS_END = OFF_HM + (size_t)L_ * F_ * 2;

DEVI float bf2f(u16 v) { return __uint_as_float(((unsigned)v) << 16); }
DEVI u16 f2bf(float f) { __bf16 b = (__bf16)f; return __builtin_bit_cast(u16, b); }
DEVI float rbf(float f) { return bf2f(f2bf(f)); }
DEVI unsigned pack2(float a, float b) { bf16x2_t v; v[0] = (__bf16)a; v[1] = (__bf16)b; return __builtin_bit_cast(unsigned, v); }
DEVI float lo16(unsigned w) { return __uint_as_float(w << 16); }
DEVI float hi16(unsigned w) { return __uint_as_float(w & 0xffff0000u); }

DEVI void glds16(const void* gsrc, unsigned lds_dst) {
  unsigned keep;
  asm volatile("s_mov_b32 %0, m0\n\ts_mov_b32 m0, %2\n\ts_nop 0\n\tglobal_load_lds_dwordx4 %1, off\n\ts_mov_b32 m0, %0"
               : "=&s"(keep) : "v"(gsrc), "s"(lds_dst) : "memory");
}

struct cf { float re, im; };
DEVI cf cmul(cf a, cf b) { cf r; r.re = a.re * b.re - a.im * b.im; r.im = a.re * b.im + a.im * b.re; return r; }
DEVI cf csq(cf a) { cf r; r.re = a.re * a.re - a.im * a.im; r.im = 2.f * a.re * a.im; return r; }
DEVI cf cexp_(float re, float im) {
  float e = __expf(re);
  const float CH = 0.15915493667125702f, CL = 6.420638316725915e-09f;
  float hi = im * CH;
  float lo = fmaf(im, CH, -hi) + im * CL;
  float r = __builtin_amdgcn_fractf(hi) + lo;
  cf o; o.re = e * __builtin_amdgcn_cosf(r); o.im = e * __builtin_amdgcn_sinf(r);
  return o;
}

DEVI cf cexp2_(float re2, float rev) {
  float e = __builtin_amdgcn_exp2f(re2);
  float r = __builtin_amdgcn_fractf(rev);
  cf o; o.re = e * __builtin_amdgcn_cosf(r); o.im = e * __builtin_amdgcn_sinf(r);
  return o;
}

DEVI float sum16(float v) {
  v += __shfl_xor(v, 1); v += __shfl_xor(v, 2); v += __shfl_xor(v, 4); v += __shfl_xor(v, 8);
  return v;
}

DEVI void ln_row40(const float (&v)[40], float (&o)[40]) {
  float s = 0.f;
#pragma unroll
  for (int i = 0; i < 40; ++i) s += v[i];
  float mean = sum16(s) * (1.f / 640.f);
  float q = 0.f;
#pragma unroll
  for (int i = 0; i < 40; ++i) { float t = v[i] - mean; q = fmaf(t, t, q); }
  float var = sum16(q) * (1.f / 640.f);
  float mb = rbf(mean);
  float sd = rbf(sqrtf(rbf(rbf(var) + 1e-5f)));
#pragma unroll
  for (int i = 0; i < 40; ++i) o[i] = rbf(v[i] - mb) / sd;
}

typedef __attribute__((ext_vector_type(2))) float f2;
struct cf2 { f2 re, im; };
DEVI cf2 cmul2(cf2 a, cf2 b) { cf2 r; r.re = a.re * b.re - a.im * b.im; r.im = a.re * b.im + a.im * b.re; return r; }
DEVI cf2 csq2(cf2 a) { cf2 r; r.re = a.re * a.re - a.im * a.im; r.im = (a.re + a.re) * a.im; return r; }
DEVI cf2 cexp2_2(f2 re2, f2 rev) {
  f2 e, c, sn;
  e.x = __builtin_amdgcn_exp2f(re2.x); e.y = __builtin_amdgcn_exp2f(re2.y);
  const float r0 = __builtin_amdgcn_fractf(rev.x), r1 = __builtin_amdgcn_fractf(rev.y);
  c.x = __builtin_amdgcn_cosf(r0); c.y = __builtin_amdgcn_cosf(r1);
  sn.x = __builtin_amdgcn_sinf(r0); sn.y = __builtin_amdgcn_sinf(r1);
  cf2 o; o.re = e * c; o.im = e * sn;
  return o;
}
__global__ __launch_bounds__(256) void k1_ln1_kgen(const u16* __restrict__ x, const u16* __restrict__ lAr,
                                                   const u16* __restrict__ Aim, const u16* __restrict__ Cre,
                                                   const u16* __restrict__ Cim, const u16* __restrict__ ldt,
                                                   u16* __restrict__ uT, u16* __restrict__ Kimg) {
  __shared__ __attribute__((aligned(16))) char sm[20480];
  const int tid = threadIdx.x, w = tid >> 6, lane = tid & 63;
  if (blockIdx.x < 128) {
    const int l0 = blockIdx.x * 16;
    const int r = w * 4 + (lane >> 4), c = lane & 15;
    const u16* xr = x + (size_t)(l0 + r) * D_;
    u32x4 raw[5];
#pragma unroll
    for (int i = 0; i < 5; ++i) raw[i] = *(const u32x4*)(xr + 8 * (c + 16 * i));
    float v[40], o[40];
#pragma unroll
    for (int i = 0; i < 5; ++i)
#pragma unroll
      for (int j = 0; j < 4; ++j) { v[8 * i + 2 * j] = lo16(raw[i][j]); v[8 * i + 2 * j + 1] = hi16(raw[i][j]); }
    ln_row40(v, o);
    u16* tile = (u16*)sm;
#pragma unroll
    for (int i = 0; i < 5; ++i) {
      u32x4 p;
#pragma unroll
      for (int j = 0; j < 4; ++j) p[j] = pack2(o[8 * i + 2 * j], o[8 * i + 2 * j + 1]);
      *(u32x4*)(tile + r * D_ + 8 * (c + 16 * i)) = p;
    }
    __syncthreads();
#pragma unroll
    for (int it = 0; it < 5; ++it) {
      const int e = tid + 256 * it, d = e % D_, q = e / D_;
      unsigned pk[4];
#pragma unroll
      for (int j = 0; j < 4; ++j) pk[j] = (unsigned)tile[(8 * q + 2 * j) * D_ + d] | ((unsigned)tile[(8 * q + 2 * j + 1) * D_ + d] << 16);
      *(u32x4*)(uT + (size_t)d * L_ + l0 + 8 * q) = (u32x4){pk[0], pk[1], pk[2], pk[3]};
    }
    return;
  }
  const int d = blockIdx.x - 128;
  const int xi = lane & 15, kq = lane >> 4;
  float (*red)[4][256] = (float (*)[4][256])sm;
  float* cst = (float*)(sm + 16384);
  if (tid < N_) {
    const float dt = rbf(expf(bf2f(ldt[d])));
    const float ar = -rbf(expf(bf2f(lAr[d * N_ + tid]))) * dt;
    const float ai = bf2f(Aim[d * N_ + tid]) * dt;
    const cf G = cexp_(ar * 128.f, ai * 128.f);
    float* cp = cst + (tid >> 1) * 12 + (tid & 1);
    cp[0] = ar * 1.4426950408889634f; cp[2] = ai * 0.15915494309189535f;
    cp[4] = 2.f * (bf2f(Cre[d * N_ + tid]) * dt); cp[6] = 2.f * (bf2f(Cim[d * N_ + tid]) * dt);
    cp[8] = G.re; cp[10] = G.im;
  }
  __syncthreads();
  const float px = (float)xi;
  f32x4 acc[4], acd[4];
#pragma unroll
  for (int s = 0; s < 4; ++s) { acc[s] = (f32x4){0.f, 0.f, 0.f, 0.f}; acd[s] = (f32x4){0.f, 0.f, 0.f, 0.f}; }
#pragma unroll
  for (int hf = 0; hf < 2; ++hf) {
    cf2 Ms[2][4], Vs[2][4];
#pragma unroll
    for (int tp = 0; tp < 2; ++tp) {
      const int pr = 16 * w + 8 * hf + 4 * tp + kq;
      const f32x4 c0 = *(const f32x4*)(cst + pr * 12);
      const f32x4 c1 = *(const f32x4*)(cst + pr * 12 + 4);
      const f32x4 c2 = *(const f32x4*)(cst + pr * 12 + 8);
      const f2 ar2 = {c0[0], c0[1]}, ai2 = {c0[2], c0[3]};
      cf2 C; C.re = (f2){c1[0], c1[1]}; C.im = (f2){c1[2], c1[3]};
      cf2 G; G.re = (f2){c2[0], c2[1]}; G.im = (f2){c2[2], c2[3]};
      cf2 V = cexp2_2(ar2 * px, ai2 * px);
      cf2 E0 = cexp2_2(ar2 * (16.f * px), ai2 * (16.f * px));
      cf2 F = cmul2(E0, G);
      Ms[tp][0] = cmul2(C, E0); Vs[tp][0] = V;
#pragma unroll
      for (int s = 1; s < 4; ++s) { F = csq2(F); V = csq2(V); Ms[tp][s] = cmul2(C, F); Vs[tp][s] = V; }
    }
    __builtin_amdgcn_sched_barrier(0);
#pragma unroll
    for (int tp = 0; tp < 2; ++tp)
#pragma unroll
      for (int s = 0; s < 4; ++s) {
        acc[s] = __builtin_amdgcn_mfma_f32_16x16x4f32(Ms[tp][s].re.x, Vs[tp][s].re.x, acc[s], 0, 0, 0);
        acd[s] = __builtin_amdgcn_mfma_f32_16x16x4f32(Ms[tp][s].im.x, Vs[tp][s].im.x, acd[s], 0, 0, 0);
      }
#pragma unroll
    for (int tp = 0; tp < 2; ++tp)
#pragma unroll
      for (int s = 0; s < 4; ++s) {
        acc[s] = __builtin_amdgcn_mfma_f32_16x16x4f32(Ms[tp][s].re.y, Vs[tp][s].re.y, acc[s], 0, 0, 0);
        acd[s] = __builtin_amdgcn_mfma_f32_16x16x4f32(Ms[tp][s].im.y, Vs[tp][s].im.y, acd[s], 0, 0, 0);
      }
    __builtin_amdgcn_sched_barrier(0);
  }
#pragma unroll
  for (int s = 0; s < 4; ++s)
#pragma unroll
    for (int r = 0; r < 4; ++r) red[w][s][(4 * kq + r) * 16 + xi] = acc[s][r] - acd[s][r];
  __syncthreads();
  float* tabl = (float*)(sm + 16384);
  {
    const f32x4* rf = (const f32x4*)sm;
    f32x4 t = (rf[tid] + rf[256 + tid]) + (rf[512 + tid] + rf[768 + tid]);
    ((f32x4*)tabl)[tid] = t;
  }
  __syncthreads();
  {
    const int g = tid;
    float kv[8];
    if (g < 32) {
#pragma unroll
      for (int e = 0; e < 8; ++e) kv[e] = tabl[8 * g + e];
    } else if (g < 64) {
      const float* t = tabl + 256 + 4 * g - 128;
      const float t0 = t[0], t1 = t[1], t2 = t[2], t3 = t[3], t4 = (g == 63) ? tabl[512] : t[4];
      kv[0] = t0; kv[1] = t0; kv[2] = t1; kv[3] = t2; kv[4] = t2; kv[5] = t2; kv[6] = t3; kv[7] = t4;
    } else if (g < 128) {
      const float* t = tabl + 512 + 2 * g - 128;
      const float t0 = t[0], t1 = t[1], t2 = (g == 127) ? tabl[768] : t[2];
      kv[0] = t0; kv[1] = t0; kv[2] = t0; kv[3] = t1; kv[4] = t1; kv[5] = t1; kv[6] = t2; kv[7] = t2;
    } else {
      const float* t = tabl + 768 + g - 128;
      const float t0 = t[0], t1 = t[1];
      kv[0] = t0; kv[1] = t0; kv[2] = t0; kv[3] = t0; kv[4] = (g & 1) ? t1 : t0; kv[5] = t1; kv[6] = t1; kv[7] = t1;
    }
    u32x4 hi, lo;
#pragma unroll
    for (int j = 0; j < 4; ++j) {
      const unsigned h = pack2(kv[7 - 2 * j], kv[6 - 2 * j]);
      hi[j] = h;
      lo[j] = pack2(kv[7 - 2 * j] - lo16(h), kv[6 - 2 * j] - hi16(h));
    }
    u16* dst = Kimg + (size_t)d * (2 * L_);
    *(u32x4*)(dst + 8 * g) = hi;
    *(u32x4*)(dst + L_ + 8 * g) = lo;
  }
}

constexpr int CV_CS = 4160;
constexpr int CV_UB = 8 * CV_CS;
constexpr int CV_KP = 95 * 80;
__global__ __launch_bounds__(256, 3) void k2_conv(const u16* __restrict__ uT, const u16* __restrict__ Kimg,
                                               const u16* __restrict__ Dskip, u16* __restrict__ sT) {
  __shared__ __attribute__((aligned(16))) char sm[CV_UB + 2 * CV_KP];
  const int tid = threadIdx.x, w = tid >> 6, lane = tid & 63;
  const int d = blockIdx.x;
  const u16* ug = uT + (size_t)d * L_;
  u32x4 cur = *(const u32x4*)(ug + 8 * tid);
  u32x4 prv = (u32x4){0u, 0u, 0u, 0u};
  if (tid > 0) prv = *(const u32x4*)(ug + 8 * (tid - 1));
  const u16* kg = Kimg + (size_t)d * (2 * L_);
  u32x4 kh = *(const u32x4*)(kg + 8 * tid);
  u32x4 kl = *(const u32x4*)(kg + L_ + 8 * tid);
  if (tid < 24) *(u32x4*)(sm + (tid / 3) * CV_CS + 16 * (tid % 3)) = (u32x4){0u, 0u, 0u, 0u};
  for (int i = tid; i < 2 * 155; i += 256) {
    int p = i / 155, o = i % 155;
    *(u32x4*)(sm + CV_UB + p * CV_KP + 16 * o) = (u32x4){0u, 0u, 0u, 0u};
  }
  {
    unsigned wd[8] = {prv[0], prv[1], prv[2], prv[3], cur[0], cur[1], cur[2], cur[3]};
#pragma unroll
    for (int k = 0; k < 8; ++k) {
      u32x4 o;
      if (k & 1) {
#pragma unroll
        for (int j = 0; j < 4; ++j) o[j] = wd[(k + 1) / 2 + j];
      } else {
#pragma unroll
        for (int j = 0; j < 4; ++j) o[j] = __builtin_amdgcn_alignbit(wd[k / 2 + j + 1], wd[k / 2 + j], 16);
      }
      *(u32x4*)(sm + k * CV_CS + 16 * (tid + 3)) = o;
    }
    const int col = tid >> 2, q = tid & 3;
    *(u32x4*)(sm + CV_UB + (col + 31) * 80 + 16 * q) = kh;
    *(u32x4*)(sm + CV_UB + CV_KP + (col + 31) * 80 + 16 * q) = kl;
  }
  __syncthreads();
  const int b = lane & 31, h = lane >> 5;
  f32x16 acc0, acc1;
#pragma unroll
  for (int i = 0; i < 16; ++i) { acc0[i] = 0.f; acc1[i] = 0.f; }
  const char* abase = sm + (b & 7) * CV_CS + 16 * ((b >> 3) - h + 3);
  const char* bbase = sm + CV_UB + (b + 31) * 80 + 16 * h;
  {
    const char* ap = abase + 64 * w;
    const char* bp = bbase - 80 * w;
#pragma unroll
    for (int it = 0; it < 8; ++it) {
#pragma unroll
      for (int s = 0; s < 2; ++s) {
        bf16x8 a = *(const bf16x8*)(ap + 256 * it - 32 * s);
        bf16x8 b1h = *(const bf16x8*)(bp + 80 * (32 - 4 * it) + 32 * s);
        bf16x8 b1l = *(const bf16x8*)(bp + 80 * (32 - 4 * it) + 32 * s + CV_KP);
        bf16x8 b0h = *(const bf16x8*)(bp - 80 * 4 * it + 32 * s);
        bf16x8 b0l = *(const bf16x8*)(bp - 80 * 4 * it + 32 * s + CV_KP);
        acc1 = __builtin_amdgcn_mfma_f32_32x32x16_bf16(a, b1h, acc1, 0, 0, 0);
        acc0 = __builtin_amdgcn_mfma_f32_32x32x16_bf16(a, b0h, acc0, 0, 0, 0);
        acc1 = __builtin_amdgcn_mfma_f32_32x32x16_bf16(a, b1l, acc1, 0, 0, 0);
        acc0 = __builtin_amdgcn_mfma_f32_32x32x16_bf16(a, b0l, acc0, 0, 0, 0);
      }
    }
#pragma unroll
    for (int it = 8; it < 16; ++it) {
#pragma unroll
      for (int s = 0; s < 2; ++s) {
        bf16x8 a = *(const bf16x8*)(ap + 256 * it - 32 * s);
        bf16x8 b1h = *(const bf16x8*)(bp + 80 * (32 - 4 * it) + 32 * s);
        bf16x8 b1l = *(const bf16x8*)(bp + 80 * (32 - 4 * it) + 32 * s + CV_KP);
        acc1 = __builtin_amdgcn_mfma_f32_32x32x16_bf16(a, b1h, acc1, 0, 0, 0);
        acc1 = __builtin_amdgcn_mfma_f32_32x32x16_bf16(a, b1l, acc1, 0, 0, 0);
      }
    }
  }
  __syncthreads();
  float* red = (float*)sm;
#pragma unroll
  for (int g = 0; g < 4; ++g) {
    f32x4 v0 = {acc0[4 * g], acc0[4 * g + 1], acc0[4 * g + 2], acc0[4 * g + 3]};
    f32x4 v1 = {acc1[4 * g], acc1[4 * g + 1], acc1[4 * g + 2], acc1[4 * g + 3]};
    *(f32x4*)(red + w * 2304 + b * 36 + 8 * g + 4 * h) = v0;
    *(f32x4*)(red + w * 2304 + (32 + b) * 36 + 8 * g + 4 * h) = v1;
  }
  __syncthreads();
  {
    const int a = tid >> 2, bb = (tid & 3) * 8;
    f32x4 s0 = {0.f, 0.f, 0.f, 0.f}, s1 = {0.f, 0.f, 0.f, 0.f};
#pragma unroll
    for (int ww = 0; ww < 4; ++ww) {
      s0 += *(const f32x4*)(red + ww * 2304 + a * 36 + bb);
      s1 += *(const f32x4*)(red + ww * 2304 + a * 36 + bb + 4);
    }
    const float dsk = bf2f(Dskip[d]);
    float y[8] = {s0[0], s0[1], s0[2], s0[3], s1[0], s1[1], s1[2], s1[3]};
    u32x4 o;
#pragma unroll
    for (int j = 0; j < 4; ++j) {
      float e0 = rbf(y[2 * j]) + rbf(lo16(cur[j]) * dsk);
      float e1 = rbf(y[2 * j + 1]) + rbf(hi16(cur[j]) * dsk);
      o[j] = pack2(e0, e1);
    }
    *(u32x4*)(sT + (size_t)d * L_ + 8 * tid) = o;
  }
}

__global__ __launch_bounds__(256) void k3_ln2_retile(const u16* __restrict__ x, const u16* __restrict__ sT,
                                                     const u16* __restrict__ wg, const u16* __restrict__ wu,
                                                     const u16* __restrict__ wd, u16* __restrict__ hbuf,
                                                     u16* __restrict__ zf, u16* __restrict__ W1t, u16* __restrict__ W2t) {
  __shared__ __attribute__((aligned(16))) u16 tile[16 * D_];
  const int tid = threadIdx.x, w = tid >> 6, lane = tid & 63;
  if (blockIdx.x < 128) {
    const int l0 = blockIdx.x * 16;
    u32x4 sv[5];
#pragma unroll
    for (int i = 0; i < 5; ++i) {
      const int c = tid + 256 * i;
      sv[i] = *(const u32x4*)(sT + (size_t)(c >> 1) * L_ + l0 + 8 * (c & 1));
    }
    const int r = w * 4 + (lane >> 4), c16 = lane & 15;
    const u16* xr = x + (size_t)(l0 + r) * D_;
    u32x4 raw[5];
#pragma unroll
    for (int i = 0; i < 5; ++i) raw[i] = *(const u32x4*)(xr + 8 * (c16 + 16 * i));
#pragma unroll
    for (int i = 0; i < 5; ++i) {
      const int c = tid + 256 * i;
      const int dd = c >> 1, r0 = 8 * (c & 1);
#pragma unroll
      for (int j = 0; j < 4; ++j) {
        tile[(r0 + 2 * j) * D_ + dd] = (u16)(sv[i][j] & 0xffffu);
        tile[(r0 + 2 * j + 1) * D_ + dd] = (u16)(sv[i][j] >> 16);
      }
    }
    __syncthreads();
    float v[40], o[40];
#pragma unroll
    for (int i = 0; i < 5; ++i) {
      u32x4 sr = *(const u32x4*)(tile + r * D_ + 8 * (c16 + 16 * i));
      u32x4 hp;
#pragma unroll
      for (int j = 0; j < 4; ++j) {
        float h0 = rbf(lo16(raw[i][j]) + lo16(sr[j]));
        float h1 = rbf(hi16(raw[i][j]) + hi16(sr[j]));
        v[8 * i + 2 * j] = h0; v[8 * i + 2 * j + 1] = h1;
        hp[j] = pack2(h0, h1);
      }
      *(u32x4*)(hbuf + (size_t)(l0 + r) * D_ + 8 * (c16 + 16 * i)) = hp;
    }
    ln_row40(v, o);
#pragma unroll
    for (int i = 0; i < 5; ++i) {
      u32x4 p;
#pragma unroll
      for (int j = 0; j < 4; ++j) p[j] = pack2(o[8 * i + 2 * j], o[8 * i + 2 * j + 1]);
      const int ch = c16 + 16 * i;
      const int ks = ch >> 2, kq = ch & 3;
      *(u32x4*)(zf + ((size_t)(blockIdx.x * KS1 + ks) * 64 + kq * 16 + r) * 8) = p;
    }
    return;
  }
  {
    const int unit = (blockIdx.x - 128) * 4 + w;
    const u16* src; u16* dst; int ld;
    if (unit < 1080) {
      const int mat = unit & 1, grp = (unit >> 1) % 27, ks = unit / 54;
      src = (mat ? wu : wg) + (size_t)(32 * ks) * F_ + 64 * grp; ld = F_;
      dst = W1t + ((size_t)((4 * grp) * (2 * KS1) + 2 * ks + mat)) * 512;
    } else {
      const int u2 = unit - 1080, grp = u2 % 10, ks = u2 / 10;
      src = wd + (size_t)(32 * ks) * D_ + 64 * grp; ld = D_;
      dst = W2t + ((size_t)((4 * grp) * KS2 + ks)) * 512;
    }
    const int tstride = (unit < 1080) ? 2 * KS1 * 512 : KS2 * 512;
    u32x4 v[4];
#pragma unroll
    for (int i = 0; i < 4; ++i) v[i] = *(const u32x4*)(src + (size_t)(8 * i + (lane >> 3)) * ld + 8 * (lane & 7));
    char* img = (char*)tile + w * 4608;
#pragma unroll
    for (int i = 0; i < 4; ++i) *(u32x4*)(img + (8 * i + (lane >> 3)) * 144 + 16 * (lane & 7)) = v[i];
    __builtin_amdgcn_s_waitcnt(0xC07F);
    __builtin_amdgcn_wave_barrier();
    const int g = lane >> 4, q = (lane & 15) >> 2, pp = lane & 3;
    const unsigned base = (unsigned)(size_t)img + (8 * g + q) * 144 + 8 * pp;
#pragma unroll
    for (int t = 0; t < 4; ++t) {
      u32x2 r0, r1;
      asm volatile("ds_read_b64_tr_b16 %0, %2 offset:%3\n\tds_read_b64_tr_b16 %1, %2 offset:%4\n\ts_waitcnt lgkmcnt(0)"
                   : "=&v"(r0), "=&v"(r1) : "v"(base), "i"(32 * t), "i"(32 * t + 4 * 144) : "memory");
      *(u32x4*)(dst + (size_t)t * tstride + lane * 8) = (u32x4){r0[0], r0[1], r1[0], r1[1]};
    }
  }
}

DEVI float gelu_tanh(float x) {
  const float c0 = -2.f * 0.7978845608028654f * 1.4426950408889634f, c1 = 0.044715f;
  float t = x * fmaf(c1 * x, x, 1.f);
  float e = __builtin_amdgcn_exp2f(c0 * t);
  return x * __builtin_amdgcn_rcpf(1.f + e);
}

constexpr int G1_STAGE = 44 * 1024;
constexpr int G1_NBUF = 3;
__global__ __launch_bounds__(512, 2) void k4_gemm1(const u16* __restrict__ zf, const u16* __restrict__ W1t,
                                                   u16* __restrict__ hmf) {
  __shared__ __attribute__((aligned(1024))) char sm[G1_NBUF * G1_STAGE];
  const int tid = threadIdx.x, lane = tid & 63;
  const int wv = __builtin_amdgcn_readfirstlane(tid >> 6);
  const int w = wv & 3;
  const int xcd = blockIdx.x & 7, idx = blockIdx.x >> 3;
  const int mt = (xcd & 1) * 8 + (idx & 7), nc = (xcd >> 1) * 4 + (idx >> 3);
  const int t0 = 7 * nc - (nc > 11 ? nc - 11 : 0);
  if (wv >= 4) {
    const unsigned smbase = (unsigned)(size_t)sm;
    const u16* zsrc = zf + ((size_t)(mt * 8 * KS1) * 64 + lane) * 8;
    const u16* wsrc = W1t + ((size_t)(t0 * (2 * KS1)) * 64 + lane) * 8;
    auto stage = [&](int kb) {
#pragma unroll
      for (int i = 0; i < 11; ++i) {
        const int f = w + 4 * i;
        const u16* src;
        if (f < 16) src = zsrc + (size_t)((f >> 1) * KS1 + 2 * kb + (f & 1)) * 512;
        else { const int g = f - 16; src = wsrc + (size_t)((g >> 2) * (2 * KS1) + 4 * kb + (g & 3)) * 512; }
        glds16(src, smbase + (kb % G1_NBUF) * G1_STAGE + f * 1024);
      }
    };
    stage(0); stage(1);
#pragma unroll
    for (int kb = 0; kb < 10; ++kb) {
      if (kb < 9) asm volatile("s_waitcnt vmcnt(11)" ::: "memory");
      else asm volatile("s_waitcnt vmcnt(0)" ::: "memory");
      __builtin_amdgcn_s_barrier();
      asm volatile("" ::: "memory");
      if (kb + 2 < 10) stage(kb + 2);
    }
    return;
  }
  f32x4 acc[7][2][2];
#pragma unroll
  for (int t = 0; t < 7; ++t)
#pragma unroll
    for (int m = 0; m < 2; ++m)
#pragma unroll
      for (int r = 0; r < 2; ++r) acc[t][m][r] = (f32x4){0.f, 0.f, 0.f, 0.f};
#pragma unroll
  for (int kb = 0; kb < 10; ++kb) {
    __builtin_amdgcn_s_barrier();
    asm volatile("" ::: "memory");
    const char* base = sm + (kb % G1_NBUF) * G1_STAGE + lane * 16;
    bf16x8 zr[2][2];
#pragma unroll
    for (int kss = 0; kss < 2; ++kss)
#pragma unroll
      for (int r = 0; r < 2; ++r) zr[kss][r] = *(const bf16x8*)(base + ((2 * w + r) * 2 + kss) * 1024);
    bf16x8 wf[2][4];
    auto rdw = [&](int grp) {
#pragma unroll
      for (int i = 0; i < 4; ++i) {
        const int g = grp * 4 + i, kss = g / 14, t = (g % 14) >> 1, m = g & 1;
        wf[grp & 1][i] = *(const bf16x8*)(base + (16 + t * 4 + kss * 2 + m) * 1024);
      }
    };
    rdw(0);
    __builtin_amdgcn_sched_barrier(0);
#pragma unroll
    for (int grp = 0; grp < 7; ++grp) {
      if (grp < 6) rdw(grp + 1);
#pragma unroll
      for (int i = 0; i < 4; ++i) {
        const int g = grp * 4 + i, kss = g / 14, t = (g % 14) >> 1, m = g & 1;
#pragma unroll
        for (int r = 0; r < 2; ++r)
          acc[t][m][r] = __builtin_amdgcn_mfma_f32_16x16x32_bf16(wf[grp & 1][i], zr[kss][r], acc[t][m][r], 0, 0, 0);
      }
      __builtin_amdgcn_sched_group_barrier(0x100, 4, 0);
      __builtin_amdgcn_sched_group_barrier(0x008, 8, 0);
      __builtin_amdgcn_sched_barrier(0);
    }
    asm volatile("s_waitcnt lgkmcnt(0)" ::: "memory");
  }
  const int lq = lane >> 4;
#pragma unroll
  for (int t = 0; t < 7; ++t) {
    const int T = t0 + t;
    const int ks2 = T >> 1, kq = 2 * (T & 1) + (lq >> 1), j0 = 4 * (lq & 1);
#pragma unroll
    for (int r = 0; r < 2; ++r) {
      float hv[4];
#pragma unroll
      for (int j = 0; j < 4; ++j) {
        float g = rbf(acc[t][0][r][j]), u = rbf(acc[t][1][r][j]);
        hv[j] = rbf(gelu_tanh(g)) * u;
      }
      const int rt = mt * 8 + 2 * w + r;
      u32x2 o = {pack2(hv[0], hv[1]), pack2(hv[2], hv[3])};
      *(u32x2*)(hmf + ((size_t)(rt * KS2 + ks2) * 64 + kq * 16 + (lane & 15)) * 8 + j0) = o;
    }
  }
}

__global__ __launch_bounds__(256) void k5_gemm2(const u16* __restrict__ hmf, const u16* __restrict__ W2t,
                                                const u16* __restrict__ hbuf, u16* __restrict__ out) {
  __shared__ __attribute__((aligned(16))) float red[4 * 20 * 64 * 4];
  const int tid = threadIdx.x, lane = tid & 63;
  const int w = __builtin_amdgcn_readfirstlane(tid >> 6);
  const int xcd = blockIdx.x & 7, idx = blockIdx.x >> 3;
  const int tt = (xcd & 3) * 8 + (idx & 7), dg = (xcd >> 2) * 4 + (idx >> 3);
  const int ks0 = w * 14 - (w > 2 ? w - 2 : 0), cnt = w < 2 ? 14 : 13;
  f32x4 acc[5][4];
#pragma unroll
  for (int j = 0; j < 5; ++j)
#pragma unroll
    for (int i = 0; i < 4; ++i) acc[j][i] = (f32x4){0.f, 0.f, 0.f, 0.f};
  const u16* ap = hmf + ((size_t)(tt * 4) * KS2 * 64 + lane) * 8;
  const u16* bp = W2t + ((size_t)(dg * 5) * KS2 * 64 + lane) * 8;
  constexpr int RD = 5;
  u32x4 a[RD][4], b[RD][5];
  auto ld = [&](int k) {
    const int ks = ks0 + (k < cnt ? k : cnt - 1);
#pragma unroll
    for (int i = 0; i < 4; ++i) {
      const u16* p = ap + (size_t)(i * KS2 + ks) * 512;
      asm volatile("global_load_dwordx4 %0, %1, off" : "=v"(a[k % RD][i]) : "v"(p) : "memory");
    }
#pragma unroll
    for (int j = 0; j < 5; ++j) {
      const u16* p = bp + (size_t)(j * KS2 + ks) * 512;
      asm volatile("global_load_dwordx4 %0, %1, off" : "=v"(b[k % RD][j]) : "v"(p) : "memory");
    }
  };
#pragma unroll
  for (int k = 0; k < RD - 1; ++k) ld(k);
#pragma unroll
  for (int k = 0; k < 14; ++k) {
    if (k + RD - 1 < 14) ld(k + RD - 1);
    const int later = (13 - k) < (RD - 1) ? (13 - k) : (RD - 1);
    if (later == 4) asm volatile("s_waitcnt vmcnt(36)" ::: "memory");
    else if (later == 3) asm volatile("s_waitcnt vmcnt(27)" ::: "memory");
    else if (later == 2) asm volatile("s_waitcnt vmcnt(18)" ::: "memory");
    else if (later == 1) asm volatile("s_waitcnt vmcnt(9)" ::: "memory");
    else asm volatile("s_waitcnt vmcnt(0)" ::: "memory");
#pragma unroll
    for (int i = 0; i < 4; ++i) asm volatile("" : "+v"(a[k % RD][i]));
#pragma unroll
    for (int j = 0; j < 5; ++j) asm volatile("" : "+v"(b[k % RD][j]));
    if (k < 13 || cnt == 14) {
#pragma unroll
      for (int j = 0; j < 5; ++j)
#pragma unroll
        for (int i = 0; i < 4; ++i)
          acc[j][i] = __builtin_amdgcn_mfma_f32_16x16x32_bf16(__builtin_bit_cast(bf16x8, b[k % RD][j]),
                                                              __builtin_bit_cast(bf16x8, a[k % RD][i]), acc[j][i], 0, 0, 0);
    }
    __builtin_amdgcn_sched_barrier(0);
  }
#pragma unroll
  for (int j = 0; j < 5; ++j)
#pragma unroll
    for (int i = 0; i < 4; ++i) *(f32x4*)(red + ((w * 20 + j * 4 + i) * 64 + lane) * 4) = acc[j][i];
  __syncthreads();
#pragma unroll
  for (int q = 0; q < 5; ++q) {
    const int tl = w * 5 + q;
    const int j = tl >> 2, i = tl & 3;
    f32x4 s = *(const f32x4*)(red + ((0 * 20 + tl) * 64 + lane) * 4);
#pragma unroll
    for (int ww = 1; ww < 4; ++ww) s += *(const f32x4*)(red + ((ww * 20 + tl) * 64 + lane) * 4);
    const int token = tt * 64 + i * 16 + (lane & 15);
    const int dcol = dg * 80 + j * 16 + 4 * (lane >> 4);
    u32x2 hv = *(const u32x2*)(hbuf + (size_t)token * D_ + dcol);
    u32x2 o;
    o[0] = pack2(lo16(hv[0]) + rbf(s[0]), hi16(hv[0]) + rbf(s[1]));
    o[1] = pack2(lo16(hv[1]) + rbf(s[2]), hi16(hv[1]) + rbf(s[3]));
    *(u32x2*)(out + (size_t)token * D_ + dcol) = o;
  }
}

extern "C" long long dev_ws_offset(int which) {
  switch (which) {
    case 0: return (long long)OFF_UT;
    case 1: return (long long)OFF_KI;
    case 2: return (long long)OFF_ST;
    case 3: return (long long)OFF_H;
    case 4: return (long long)OFF_ZF;
    default: return -1;
  }
}

extern "C" void kernel_launch(void* const* d_in, const int* in_sizes, int n_in, void* d_out, int out_size,
                              void* d_ws, size_t ws_size, hipStream_t stream) {
  const u16* x = (const u16*)d_in[0];
  const u16* lAr = (const u16*)d_in[1];
  const u16* Aim = (const u16*)d_in[2];
  const u16* Cre = (const u16*)d_in[3];
  const u16* Cim = (const u16*)d_in[4];
  const u16* ldt = (const u16*)d_in[5];
  const u16* Dsk = (const u16*)d_in[6];
  const u16* wg = (const u16*)d_in[8];
  const u16* wu = (const u16*)d_in[9];
  const u16* wd = (const u16*)d_in[10];
  char* ws = (char*)d_ws;
  u16* uT = (u16*)(ws + OFF_UT);
  u16* Ki = (u16*)(ws + OFF_KI);
  u16* sT = (u16*)(ws + OFF_ST);
  u16* hb = (u16*)(ws + OFF_H);
  u16* zf = (u16*)(ws + OFF_ZF);
  u16* W1 = (u16*)(ws + OFF_W1);
  u16* W2 = (u16*)(ws + OFF_W2);
  u16* hm = (u16*)(ws + OFF_HM);
  k1_ln1_kgen<<<128 + 640, 256, 0, stream>>>(x, lAr, Aim, Cre, Cim, ldt, uT, Ki);
  k2_conv<<<640, 256, 0, stream>>>(uT, Ki, Dsk, sT);
  k3_ln2_retile<<<128 + 405, 256, 0, stream>>>(x, sT, wg, wu, wd, hb, zf, W1, W2);
  k4_gemm1<<<256, 512, 0, stream>>>(zf, W1, hm);
  k5_gemm2<<<256, 256, 0, stream>>>(hm, W2, hb, (u16*)d_out);
}
```

```cpp
#include <hip/hip_runtime.h>
#include <hip/hip_bf16.h>
#include <stdint.h>

typedef __attribute__((ext_vector_type(8))) short bf16x8;
typedef __attribute__((ext_vector_type(4))) float f32x4;
typedef __attribute__((ext_vector_type(16))) float f32x16;
typedef __attribute__((ext_vector_type(4))) unsigned int u32x4;
typedef __attribute__((ext_vector_type(2))) unsigned int u32x2;
typedef __attribute__((ext_vector_type(2))) __bf16 bf16x2_t;
typedef unsigned short u16;

#define DEVI __device__ __forceinline__

constexpr int L_ = 2048, D_ = 640, N_ = 128, F_ = 1728;
constexpr int KS1 = D_ / 32;
constexpr int KS2 = F_ / 32;

constexpr size_t OFF_UT = 0;
constexpr size_t OFF_KI = OFF_UT + (size_t)D_ * L_ * 2;
constexpr size_t OFF_ST = OFF_KI + (size_t)D_ * L_ * 4;
constexpr size_t OFF_H  = OFF_ST + (size_t)D_ * L_ * 2;
constexpr size_t OFF_ZF = OFF_H + (size_t)D_ * L_ * 2;
constexpr size_t OFF_W1 = OFF_ZF + (size_t)D_ * L_ * 2;
constexpr size_t OFF_W2 = OFF_W1 + (size_t)D_ * F_ * 4;
constexpr size_t OFF_HM = OFF_W2 + (size_t)D_ * F_ * 2;
constexpr size_t WS_END = OFF_HM + (size_t)L_ * F_ * 2;

DEVI float bf2f(u16 v) { return __uint_as_float(((unsigned)v) << 16); }
DEVI u16 f2bf(float f) { __bf16 b = (__bf16)f; return __builtin_bit_cast(u16, b); }
DEVI float rbf(float f) { return bf2f(f2bf(f)); }
DEVI unsigned pack2(float a, float b) { unsigned r; asm("v_cvt_pk_bf16_f32 %0, %1, %2" : "=v"(r) : "v"(a), "v"(b)); return r; }
DEVI float lo16(unsigned w) { return __uint_as_float(w << 16); }
DEVI float hi16(unsigned w) { return __uint_as_float(w & 0xffff0000u); }

DEVI void glds16(const void* gsrc, unsigned lds_dst) {
  unsigned keep;
  asm volatile("s_mov_b32 %0, m0\n\ts_mov_b32 m0, %2\n\ts_nop 0\n\tglobal_load_lds_dwordx4 %1, off\n\ts_mov_b32 m0, %0"
               : "=&s"(keep) : "v"(gsrc), "s"(lds_dst) : "memory");
}

struct cf { float re, im; };
DEVI cf cmul(cf a, cf b) { cf r; r.re = a.re * b.re - a.im * b.im; r.im = a.re * b.im + a.im * b.re; return r; }
DEVI cf csq(cf a) { cf r; r.re = a.re * a.re - a.im * a.im; r.im = 2.f * a.re * a.im; return r; }
DEVI cf cexp_(float re, float im) {
  float e = __expf(re);
  const float CH = 0.15915493667125702f, CL = 6.420638316725915e-09f;
  float hi = im * CH;
  float lo = fmaf(im, CH, -hi) + im * CL;
  float r = __builtin_amdgcn_fractf(hi) + lo;
  cf o; o.re = e * __builtin_amdgcn_cosf(r); o.im = e * __builtin_amdgcn_sinf(r);
  return o;
}

DEVI cf cexp2_(float re2, float rev) {
  float e = __builtin_amdgcn_exp2f(re2);
  float r = __builtin_amdgcn_fractf(rev);
  cf o; o.re = e * __builtin_amdgcn_cosf(r); o.im = e * __builtin_amdgcn_sinf(r);
  return o;
}

typedef __attribute__((ext_vector_type(2))) float f2;
DEVI float sum32(float v) {
  v += __shfl_xor(v, 1); v += __shfl_xor(v, 2); v += __shfl_xor(v, 4); v += __shfl_xor(v, 8); v += __shfl_xor(v, 16);
  return v;
}
DEVI f2 unpk(unsigned w) { f2 r; r.x = lo16(w); r.y = hi16(w); return r; }
DEVI unsigned pk2(f2 v) { return pack2(v.x, v.y); }
DEVI void ln_row20(const f2 (&v)[10], unsigned (&o)[10]) {
  f2 s = v[0];
#pragma unroll
  for (int i = 1; i < 10; ++i) s += v[i];
  const float mean = sum32(s.x + s.y) * (1.f / 640.f);
  f2 q = {0.f, 0.f};
#pragma unroll
  for (int i = 0; i < 10; ++i) { f2 t = v[i] - mean; q += t * t; }
  const float var = sum32(q.x + q.y) * (1.f / 640.f);
  const float mb = rbf(mean);
  const float sd = rbf(sqrtf(rbf(rbf(var) + 1e-5f)));
  const float rs = 1.f / sd;
#pragma unroll
  for (int i = 0; i < 10; ++i) {
    const f2 t = unpk(pk2(v[i] - mb));
    f2 qq = t * rs;
    qq += (t - qq * sd) * rs;
    o[i] = pk2(qq);
  }
}

struct cf2 { f2 re, im; };
DEVI cf2 cmul2(cf2 a, cf2 b) { cf2 r; r.re = a.re * b.re - a.im * b.im; r.im = a.re * b.im + a.im * b.re; return r; }
DEVI cf2 csq2(cf2 a) { cf2 r; r.re = a.re * a.re - a.im * a.im; r.im = (a.re + a.re) * a.im; return r; }
DEVI cf2 cexp2_2(f2 re2, f2 rev) {
  f2 e, c, sn;
  e.x = __builtin_amdgcn_exp2f(re2.x); e.y = __builtin_amdgcn_exp2f(re2.y);
  const float r0 = __builtin_amdgcn_fractf(rev.x), r1 = __builtin_amdgcn_fractf(rev.y);
  c.x = __builtin_amdgcn_cosf(r0); c.y = __builtin_amdgcn_cosf(r1);
  sn.x = __builtin_amdgcn_sinf(r0); sn.y = __builtin_amdgcn_sinf(r1);
  cf2 o; o.re = e * c; o.im = e * sn;
  return o;
}
__global__ __launch_bounds__(256) void k1_ln1_kgen(const u16* __restrict__ x, const u16* __restrict__ lAr,
                                                   const u16* __restrict__ Aim, const u16* __restrict__ Cre,
                                                   const u16* __restrict__ Cim, const u16* __restrict__ ldt,
                                                   u16* __restrict__ uT, u16* __restrict__ Kimg) {
  __shared__ __attribute__((aligned(16))) char sm[20480];
  const int tid = threadIdx.x, w = tid >> 6, lane = tid & 63;
  if (blockIdx.x < 128) {
    const int l0 = blockIdx.x * 16;
    const int r0 = w * 4 + (lane >> 5), c = lane & 31;
    u32x2 raw[2][5];
#pragma unroll
    for (int ps = 0; ps < 2; ++ps)
#pragma unroll
      for (int i = 0; i < 5; ++i) raw[ps][i] = *(const u32x2*)(x + (size_t)(l0 + r0 + 2 * ps) * D_ + 4 * (c + 32 * i));
    u16* tile = (u16*)sm;
#pragma unroll
    for (int ps = 0; ps < 2; ++ps) {
      f2 v[10]; unsigned o[10];
#pragma unroll
      for (int i = 0; i < 5; ++i) { v[2 * i] = unpk(raw[ps][i][0]); v[2 * i + 1] = unpk(raw[ps][i][1]); }
      ln_row20(v, o);
#pragma unroll
      for (int i = 0; i < 5; ++i) *(u32x2*)(tile + (r0 + 2 * ps) * D_ + 4 * (c + 32 * i)) = (u32x2){o[2 * i], o[2 * i + 1]};
    }
    __syncthreads();
#pragma unroll
    for (int it = 0; it < 5; ++it) {
      const int e = tid + 256 * it, d = e % D_, q = e / D_;
      unsigned pk[4];
#pragma unroll
      for (int j = 0; j < 4; ++j) pk[j] = (unsigned)tile[(8 * q + 2 * j) * D_ + d] | ((unsigned)tile[(8 * q + 2 * j + 1) * D_ + d] << 16);
      *(u32x4*)(uT + (size_t)d * L_ + l0 + 8 * q) = (u32x4){pk[0], pk[1], pk[2], pk[3]};
    }
    return;
  }
  const int d = blockIdx.x - 128;
  const int xi = lane & 15, kq = lane >> 4;
  float (*red)[4][256] = (float (*)[4][256])sm;
  float* cst = (float*)(sm + 16384);
  if (tid < N_) {
    const float dt = rbf(expf(bf2f(ldt[d])));
    const float ar = -rbf(expf(bf2f(lAr[d * N_ + tid]))) * dt;
    const float ai = bf2f(Aim[d * N_ + tid]) * dt;
    const cf G = cexp_(ar * 128.f, ai * 128.f);
    float* cp = cst + (tid >> 1) * 12 + (tid & 1);
    cp[0] = ar * 1.4426950408889634f; cp[2] = ai * 0.15915494309189535f;
    cp[4] = 2.f * (bf2f(Cre[d * N_ + tid]) * dt); cp[6] = 2.f * (bf2f(Cim[d * N_ + tid]) * dt);
    cp[8] = G.re; cp[10] = G.im;
  }
  __syncthreads();
  const float px = (float)xi;
  f32x4 acc[4], acd[4];
#pragma unroll
  for (int s = 0; s < 4; ++s) { acc[s] = (f32x4){0.f, 0.f, 0.f, 0.f}; acd[s] = (f32x4){0.f, 0.f, 0.f, 0.f}; }
#pragma unroll
  for (int hf = 0; hf < 2; ++hf) {
    cf2 Ms[2][4], Vs[2][4];
#pragma unroll
    for (int tp = 0; tp < 2; ++tp) {
      const int pr = 16 * w + 8 * hf + 4 * tp + kq;
      const f32x4 c0 = *(const f32x4*)(cst + pr * 12);
      const f32x4 c1 = *(const f32x4*)(cst + pr * 12 + 4);
      const f32x4 c2 = *(const f32x4*)(cst + pr * 12 + 8);
      const f2 ar2 = {c0[0], c0[1]}, ai2 = {c0[2], c0[3]};
      cf2 C; C.re = (f2){c1[0], c1[1]}; C.im = (f2){c1[2], c1[3]};
      cf2 G; G.re = (f2){c2[0], c2[1]}; G.im = (f2){c2[2], c2[3]};
      cf2 V = cexp2_2(ar2 * px, ai2 * px);
      cf2 E0 = cexp2_2(ar2 * (16.f * px), ai2 * (16.f * px));
      cf2 F = cmul2(E0, G);
      Ms[tp][0] = cmul2(C, E0); Vs[tp][0] = V;
#pragma unroll
      for (int s = 1; s < 4; ++s) { F = csq2(F); V = csq2(V); Ms[tp][s] = cmul2(C, F); Vs[tp][s] = V; }
    }
    __builtin_amdgcn_sched_barrier(0);
#pragma unroll
    for (int tp = 0; tp < 2; ++tp)
#pragma unroll
      for (int s = 0; s < 4; ++s) {
        acc[s] = __builtin_amdgcn_mfma_f32_16x16x4f32(Ms[tp][s].re.x, Vs[tp][s].re.x, acc[s], 0, 0, 0);
        acd[s] = __builtin_amdgcn_mfma_f32_16x16x4f32(Ms[tp][s].im.x, Vs[tp][s].im.x, acd[s], 0, 0, 0);
      }
#pragma unroll
    for (int tp = 0; tp < 2; ++tp)
#pragma unroll
      for (int s = 0; s < 4; ++s) {
        acc[s] = __builtin_amdgcn_mfma_f32_16x16x4f32(Ms[tp][s].re.y, Vs[tp][s].re.y, acc[s], 0, 0, 0);
        acd[s] = __builtin_amdgcn_mfma_f32_16x16x4f32(Ms[tp][s].im.y, Vs[tp][s].im.y, acd[s], 0, 0, 0);
      }
    __builtin_amdgcn_sched_barrier(0);
  }
#pragma unroll
  for (int s = 0; s < 4; ++s)
#pragma unroll
    for (int r = 0; r < 4; ++r) red[w][s][(4 * kq + r) * 16 + xi] = acc[s][r] - acd[s][r];
  __syncthreads();
  float* tabl = (float*)(sm + 16384);
  {
    const f32x4* rf = (const f32x4*)sm;
    f32x4 t = (rf[tid] + rf[256 + tid]) + (rf[512 + tid] + rf[768 + tid]);
    ((f32x4*)tabl)[tid] = t;
  }
  __syncthreads();
  {
    const int g = tid;
    float kv[8];
    if (g < 32) {
#pragma unroll
      for (int e = 0; e < 8; ++e) kv[e] = tabl[8 * g + e];
    } else if (g < 64) {
      const float* t = tabl + 256 + 4 * g - 128;
      const float t0 = t[0], t1 = t[1], t2 = t[2], t3 = t[3], t4 = (g == 63) ? tabl[512] : t[4];
      kv[0] = t0; kv[1] = t0; kv[2] = t1; kv[3] = t2; kv[4] = t2; kv[5] = t2; kv[6] = t3; kv[7] = t4;
    } else if (g < 128) {
      const float* t = tabl + 512 + 2 * g - 128;
      const float t0 = t[0], t1 = t[1], t2 = (g == 127) ? tabl[768] : t[2];
      kv[0] = t0; kv[1] = t0; kv[2] = t0; kv[3] = t1; kv[4] = t1; kv[5] = t1; kv[6] = t2; kv[7] = t2;
    } else {
      const float* t = tabl + 768 + g - 128;
      const float t0 = t[0], t1 = t[1];
      kv[0] = t0; kv[1] = t0; kv[2] = t0; kv[3] = t0; kv[4] = (g & 1) ? t1 : t0; kv[5] = t1; kv[6] = t1; kv[7] = t1;
    }
    u32x4 hi, lo;
#pragma unroll
    for (int j = 0; j < 4; ++j) {
      const unsigned h = pack2(kv[7 - 2 * j], kv[6 - 2 * j]);
      hi[j] = h;
      lo[j] = pack2(kv[7 - 2 * j] - lo16(h), kv[6 - 2 * j] - hi16(h));
    }
    u16* dst = Kimg + (size_t)d * (2 * L_);
    *(u32x4*)(dst + 8 * g) = hi;
    *(u32x4*)(dst + L_ + 8 * g) = lo;
  }
}

constexpr int CV_CS = 4160;
constexpr int CV_UB = 8 * CV_CS;
constexpr int CV_KP = 95 * 80;
__global__ __launch_bounds__(256, 3) void k2_conv(const u16* __restrict__ uT, const u16* __restrict__ Kimg,
                                               const u16* __restrict__ Dskip, u16* __restrict__ sT) {
  __shared__ __attribute__((aligned(16))) char sm[CV_UB + 2 * CV_KP];
  const int tid = threadIdx.x, w = tid >> 6, lane = tid & 63;
  const int d = blockIdx.x;
  const u16* ug = uT + (size_t)d * L_;
  u32x4 cur = *(const u32x4*)(ug + 8 * tid);
  u32x4 prv = (u32x4){0u, 0u, 0u, 0u};
  if (tid > 0) prv = *(const u32x4*)(ug + 8 * (tid - 1));
  const u16* kg = Kimg + (size_t)d * (2 * L_);
  u32x4 kh = *(const u32x4*)(kg + 8 * tid);
  u32x4 kl = *(const u32x4*)(kg + L_ + 8 * tid);
  if (tid < 24) *(u32x4*)(sm + (tid / 3) * CV_CS + 16 * (tid % 3)) = (u32x4){0u, 0u, 0u, 0u};
  for (int i = tid; i < 2 * 155; i += 256) {
    int p = i / 155, o = i % 155;
    *(u32x4*)(sm + CV_UB + p * CV_KP + 16 * o) = (u32x4){0u, 0u, 0u, 0u};
  }
  {
    unsigned wd[8] = {prv[0], prv[1], prv[2], prv[3], cur[0], cur[1], cur[2], cur[3]};
#pragma unroll
    for (int k = 0; k < 8; ++k) {
      u32x4 o;
      if (k & 1) {
#pragma unroll
        for (int j = 0; j < 4; ++j) o[j] = wd[(k + 1) / 2 + j];
      } else {
#pragma unroll
        for (int j = 0; j < 4; ++j) o[j] = __builtin_amdgcn_alignbit(wd[k / 2 + j + 1], wd[k / 2 + j], 16);
      }
      *(u32x4*)(sm + k * CV_CS + 16 * (tid + 3)) = o;
    }
    const int col = tid >> 2, q = tid & 3;
    *(u32x4*)(sm + CV_UB + (col + 31) * 80 + 16 * q) = kh;
    *(u32x4*)(sm + CV_UB + CV_KP + (col + 31) * 80 + 16 * q) = kl;
  }
  __syncthreads();
  const int b = lane & 31, h = lane >> 5;
  f32x16 acc0, acc1;
#pragma unroll
  for (int i = 0; i < 16; ++i) { acc0[i] = 0.f; acc1[i] = 0.f; }
  const char* abase = sm + (b & 7) * CV_CS + 16 * ((b >> 3) - h + 3);
  const char* bbase = sm + CV_UB + (b + 31) * 80 + 16 * h;
  {
    const char* ap = abase + 64 * w;
    const char* bp = bbase - 80 * w;
#pragma unroll
    for (int it = 0; it < 8; ++it) {
#pragma unroll
      for (int s = 0; s < 2; ++s) {
        bf16x8 a = *(const bf16x8*)(ap + 256 * it - 32 * s);
        bf16x8 b1h = *(const bf16x8*)(bp + 80 * (32 - 4 * it) + 32 * s);
        bf16x8 b1l = *(const bf16x8*)(bp + 80 * (32 - 4 * it) + 32 * s + CV_KP);
        bf16x8 b0h = *(const bf16x8*)(bp - 80 * 4 * it + 32 * s);
        bf16x8 b0l = *(const bf16x8*)(bp - 80 * 4 * it + 32 * s + CV_KP);
        acc1 = __builtin_amdgcn_mfma_f32_32x32x16_bf16(a, b1h, acc1, 0, 0, 0);
        acc0 = __builtin_amdgcn_mfma_f32_32x32x16_bf16(a, b0h, acc0, 0, 0, 0);
        acc1 = __builtin_amdgcn_mfma_f32_32x32x16_bf16(a, b1l, acc1, 0, 0, 0);
        acc0 = __builtin_amdgcn_mfma_f32_32x32x16_bf16(a, b0l, acc0, 0, 0, 0);
      }
    }
#pragma unroll
    for (int it = 8; it < 16; ++it) {
#pragma unroll
      for (int s = 0; s < 2; ++s) {
        bf16x8 a = *(const bf16x8*)(ap + 256 * it - 32 * s);
        bf16x8 b1h = *(const bf16x8*)(bp + 80 * (32 - 4 * it) + 32 * s);
        bf16x8 b1l = *(const bf16x8*)(bp + 80 * (32 - 4 * it) + 32 * s + CV_KP);
        acc1 = __builtin_amdgcn_mfma_f32_32x32x16_bf16(a, b1h, acc1, 0, 0, 0);
        acc1 = __builtin_amdgcn_mfma_f32_32x32x16_bf16(a, b1l, acc1, 0, 0, 0);
      }
    }
  }
  __syncthreads();
  float* red = (float*)sm;
#pragma unroll
  for (int g = 0; g < 4; ++g) {
    f32x4 v0 = {acc0[4 * g], acc0[4 * g + 1], acc0[4 * g + 2], acc0[4 * g + 3]};
    f32x4 v1 = {acc1[4 * g], acc1[4 * g + 1], acc1[4 * g + 2], acc1[4 * g + 3]};
    *(f32x4*)(red + w * 2304 + b * 36 + 8 * g + 4 * h) = v0;
    *(f32x4*)(red + w * 2304 + (32 + b) * 36 + 8 * g + 4 * h) = v1;
  }
  __syncthreads();
  {
    const int a = tid >> 2, bb = (tid & 3) * 8;
    f32x4 s0 = {0.f, 0.f, 0.f, 0.f}, s1 = {0.f, 0.f, 0.f, 0.f};
#pragma unroll
    for (int ww = 0; ww < 4; ++ww) {
      s0 += *(const f32x4*)(red + ww * 2304 + a * 36 + bb);
      s1 += *(const f32x4*)(red + ww * 2304 + a * 36 + bb + 4);
    }
    const float dsk = bf2f(Dskip[d]);
    float y[8] = {s0[0], s0[1], s0[2], s0[3], s1[0], s1[1], s1[2], s1[3]};
    u32x4 o;
#pragma unroll
    for (int j = 0; j < 4; ++j) {
      float e0 = rbf(y[2 * j]) + rbf(lo16(cur[j]) * dsk);
      float e1 = rbf(y[2 * j + 1]) + rbf(hi16(cur[j]) * dsk);
      o[j] = pack2(e0, e1);
    }
    *(u32x4*)(sT + ((size_t)(tid >> 1) * D_ + d) * 16 + 8 * (tid & 1)) = o;
  }
}

__global__ __launch_bounds__(256) void k3_ln2_retile(const u16* __restrict__ x, const u16* __restrict__ sT,
                                                     const u16* __restrict__ wg, const u16* __restrict__ wu,
                                                     const u16* __restrict__ wd, u16* __restrict__ hbuf,
                                                     u16* __restrict__ zf, u16* __restrict__ W1t, u16* __restrict__ W2t) {
  __shared__ __attribute__((aligned(16))) u16 tile[16 * D_];
  const int tid = threadIdx.x, w = tid >> 6, lane = tid & 63;
  if (blockIdx.x < 128) {
    const int l0 = blockIdx.x * 16;
    u32x4 sv[5];
#pragma unroll
    for (int i = 0; i < 5; ++i) {
      const int e = tid + 256 * i;
      sv[i] = *(const u32x4*)(sT + (size_t)blockIdx.x * (D_ * 16) + 8 * e);
    }
    const int r0 = w * 4 + (lane >> 5), c = lane & 31;
    u32x2 raw[2][5];
#pragma unroll
    for (int ps = 0; ps < 2; ++ps)
#pragma unroll
      for (int i = 0; i < 5; ++i) raw[ps][i] = *(const u32x2*)(x + (size_t)(l0 + r0 + 2 * ps) * D_ + 4 * (c + 32 * i));
#pragma unroll
    for (int i = 0; i < 5; ++i) {
      const int e = tid + 256 * i;
      const int dd = e >> 1, rr = 8 * (e & 1);
#pragma unroll
      for (int j = 0; j < 4; ++j) {
        tile[(rr + 2 * j) * D_ + dd] = (u16)(sv[i][j] & 0xffffu);
        tile[(rr + 2 * j + 1) * D_ + dd] = (u16)(sv[i][j] >> 16);
      }
    }
    __syncthreads();
#pragma unroll
    for (int ps = 0; ps < 2; ++ps) {
      const int r = r0 + 2 * ps;
      f2 v[10]; unsigned o[10];
#pragma unroll
      for (int i = 0; i < 5; ++i) {
        const u32x2 sr = *(const u32x2*)(tile + r * D_ + 4 * (c + 32 * i));
        const unsigned h0 = pk2(unpk(raw[ps][i][0]) + unpk(sr[0])), h1 = pk2(unpk(raw[ps][i][1]) + unpk(sr[1]));
        v[2 * i] = unpk(h0); v[2 * i + 1] = unpk(h1);
        *(u32x2*)(hbuf + (size_t)(l0 + r) * D_ + 4 * (c + 32 * i)) = (u32x2){h0, h1};
      }
      ln_row20(v, o);
#pragma unroll
      for (int i = 0; i < 5; ++i) {
        const int e = 4 * (c + 32 * i);
        const int ks = e >> 5, kq = (e >> 3) & 3, j0 = e & 7;
        *(u32x2*)(zf + ((size_t)(blockIdx.x * KS1 + ks) * 64 + kq * 16 + r) * 8 + j0) = (u32x2){o[2 * i], o[2 * i + 1]};
      }
    }
    return;
  }
  {
    const int unit = (blockIdx.x - 128) * 4 + w;
    const u16* src; u16* dst; int ld;
    if (unit < 1080) {
      const int mat = unit & 1, grp = (unit >> 1) % 27, ks = unit / 54;
      src = (mat ? wu : wg) + (size_t)(32 * ks) * F_ + 64 * grp; ld = F_;
      dst = W1t + ((size_t)((4 * grp) * (2 * KS1) + 2 * ks + mat)) * 512;
    } else {
      const int u2 = unit - 1080, grp = u2 % 10, ks = u2 / 10;
      src = wd + (size_t)(32 * ks) * D_ + 64 * grp; ld = D_;
      dst = W2t + ((size_t)((4 * grp) * KS2 + ks)) * 512;
    }
    const int tstride = (unit < 1080) ? 2 * KS1 * 512 : KS2 * 512;
    u32x4 v[4];
#pragma unroll
    for (int i = 0; i < 4; ++i) v[i] = *(const u32x4*)(src + (size_t)(8 * i + (lane >> 3)) * ld + 8 * (lane & 7));
    char* img = (char*)tile + w * 4608;
#pragma unroll
    for (int i = 0; i < 4; ++i) *(u32x4*)(img + (8 * i + (lane >> 3)) * 144 + 16 * (lane & 7)) = v[i];
    __builtin_amdgcn_s_waitcnt(0xC07F);
    __builtin_amdgcn_wave_barrier();
    const int g = lane >> 4, q = (lane & 15) >> 2, pp = lane & 3;
    const unsigned base = (unsigned)(size_t)img + (8 * g + q) * 144 + 8 * pp;
#pragma unroll
    for (int t = 0; t < 4; ++t) {
      u32x2 r0, r1;
      asm volatile("ds_read_b64_tr_b16 %0, %2 offset:%3\n\tds_read_b64_tr_b16 %1, %2 offset:%4\n\ts_waitcnt lgkmcnt(0)"
                   : "=&v"(r0), "=&v"(r1) : "v"(base), "i"(32 * t), "i"(32 * t + 4 * 144) : "memory");
      *(u32x4*)(dst + (size_t)t * tstride + lane * 8) = (u32x4){r0[0], r0[1], r1[0], r1[1]};
    }
  }
}

DEVI f2 gelu_tanh2(f2 x) {
  const float c0 = -2.f * 0.7978845608028654f * 1.4426950408889634f, c1 = 0.044715f;
  const f2 a = x * ((x * x) * (c0 * c1) + c0);
  f2 e; e.x = __builtin_amdgcn_exp2f(a.x); e.y = __builtin_amdgcn_exp2f(a.y);
  e += 1.f;
  f2 r; r.x = __builtin_amdgcn_rcpf(e.x); r.y = __builtin_amdgcn_rcpf(e.y);
  return x * r;
}

constexpr int G1_STAGE = 44 * 1024;
constexpr int G1_NBUF = 3;
__global__ __launch_bounds__(512, 2) void k4_gemm1(const u16* __restrict__ zf, const u16* __restrict__ W1t,
                                                   u16* __restrict__ hmf) {
  __shared__ __attribute__((aligned(1024))) char sm[G1_NBUF * G1_STAGE];
  const int tid = threadIdx.x, lane = tid & 63;
  const int wv = __builtin_amdgcn_readfirstlane(tid >> 6);
  const int w = wv & 3;
  const int xcd = blockIdx.x & 7, idx = blockIdx.x >> 3;
  const int mt = (xcd & 1) * 8 + (idx & 7), nc = (xcd >> 1) * 4 + (idx >> 3);
  const int t0 = 7 * nc - (nc > 11 ? nc - 11 : 0);
  if (wv >= 4) {
    const unsigned smbase = (unsigned)(size_t)sm;
    const u16* zsrc = zf + ((size_t)(mt * 8 * KS1) * 64 + lane) * 8;
    const u16* wsrc = W1t + ((size_t)(t0 * (2 * KS1)) * 64 + lane) * 8;
    auto stage = [&](int kb, int buf) {
#pragma unroll
      for (int i = 0; i < 11; ++i) {
        const int f = w + 4 * i;
        const u16* src;
        if (f < 16) src = zsrc + (size_t)((f >> 1) * KS1 + 2 * kb + (f & 1)) * 512;
        else { const int g = f - 16; src = wsrc + (size_t)((g >> 2) * (2 * KS1) + 4 * kb + (g & 3)) * 512; }
        glds16(src, smbase + buf * G1_STAGE + f * 1024);
      }
    };
    stage(0, 0); stage(1, 1);
    int nb = 2;
#pragma unroll 1
    for (int kb = 0; kb < 8; ++kb) {
      asm volatile("s_waitcnt vmcnt(11)" ::: "memory");
      __builtin_amdgcn_s_barrier();
      asm volatile("" ::: "memory");
      stage(kb + 2, nb);
      nb = (nb == G1_NBUF - 1) ? 0 : nb + 1;
    }
    asm volatile("s_waitcnt vmcnt(11)" ::: "memory");
    __builtin_amdgcn_s_barrier();
    asm volatile("s_waitcnt vmcnt(0)" ::: "memory");
    __builtin_amdgcn_s_barrier();
    return;
  }
  f32x4 acc[7][2][2];
#pragma unroll
  for (int t = 0; t < 7; ++t)
#pragma unroll
    for (int m = 0; m < 2; ++m)
#pragma unroll
      for (int r = 0; r < 2; ++r) acc[t][m][r] = (f32x4){0.f, 0.f, 0.f, 0.f};
  int cb = 0;
#pragma unroll 1
  for (int kb = 0; kb < 10; ++kb) {
    __builtin_amdgcn_s_barrier();
    asm volatile("" ::: "memory");
    const char* base = sm + cb * G1_STAGE + lane * 16;
    cb = (cb == G1_NBUF - 1) ? 0 : cb + 1;
    bf16x8 zr[2][2];
#pragma unroll
    for (int kss = 0; kss < 2; ++kss)
#pragma unroll
      for (int r = 0; r < 2; ++r) zr[kss][r] = *(const bf16x8*)(base + ((2 * w + r) * 2 + kss) * 1024);
    bf16x8 wf[2][4];
    auto rdw = [&](int grp) {
#pragma unroll
      for (int i = 0; i < 4; ++i) {
        const int g = grp * 4 + i, kss = g / 14, t = (g % 14) >> 1, m = g & 1;
        wf[grp & 1][i] = *(const bf16x8*)(base + (16 + t * 4 + kss * 2 + m) * 1024);
      }
    };
    rdw(0);
    __builtin_amdgcn_sched_barrier(0);
#pragma unroll
    for (int grp = 0; grp < 7; ++grp) {
      if (grp < 6) rdw(grp + 1);
#pragma unroll
      for (int i = 0; i < 4; ++i) {
        const int g = grp * 4 + i, kss = g / 14, t = (g % 14) >> 1, m = g & 1;
#pragma unroll
        for (int r = 0; r < 2; ++r)
          acc[t][m][r] = __builtin_amdgcn_mfma_f32_16x16x32_bf16(wf[grp & 1][i], zr[kss][r], acc[t][m][r], 0, 0, 0);
      }
      __builtin_amdgcn_sched_group_barrier(0x100, 4, 0);
      __builtin_amdgcn_sched_group_barrier(0x008, 8, 0);
      __builtin_amdgcn_sched_barrier(0);
    }
    asm volatile("s_waitcnt lgkmcnt(0)" ::: "memory");
  }
  const int lq = lane >> 4;
#pragma unroll
  for (int t = 0; t < 7; ++t) {
    const int T = t0 + t;
    const int ks2 = T >> 1, kq = 2 * (T & 1) + (lq >> 1), j0 = 4 * (lq & 1);
#pragma unroll
    for (int r = 0; r < 2; ++r) {
      unsigned ho[2];
#pragma unroll
      for (int j = 0; j < 2; ++j) {
        const f2 g = unpk(pack2(acc[t][0][r][2 * j], acc[t][0][r][2 * j + 1]));
        const f2 u = unpk(pack2(acc[t][1][r][2 * j], acc[t][1][r][2 * j + 1]));
        ho[j] = pk2(unpk(pk2(gelu_tanh2(g))) * u);
      }
      const int rt = mt * 8 + 2 * w + r;
      u32x2 o = {ho[0], ho[1]};
      *(u32x2*)(hmf + ((size_t)(rt * KS2 + ks2) * 64 + kq * 16 + (lane & 15)) * 8 + j0) = o;
    }
  }
}

__global__ __launch_bounds__(256) void k5_gemm2(const u16* __restrict__ hmf, const u16* __restrict__ W2t,
                                                const u16* __restrict__ hbuf, u16* __restrict__ out) {
  __shared__ __attribute__((aligned(16))) float red[4 * 20 * 64 * 4];
  const int tid = threadIdx.x, lane = tid & 63;
  const int w = __builtin_amdgcn_readfirstlane(tid >> 6);
  const int xcd = blockIdx.x & 7, idx = blockIdx.x >> 3;
  const int tt = (xcd & 3) * 8 + (idx & 7), dg = (xcd >> 2) * 4 + (idx >> 3);
  const int ks0 = w * 14 - (w > 2 ? w - 2 : 0), cnt = w < 2 ? 14 : 13;
  f32x4 acc[5][4];
#pragma unroll
  for (int j = 0; j < 5; ++j)
#pragma unroll
    for (int i = 0; i < 4; ++i) acc[j][i] = (f32x4){0.f, 0.f, 0.f, 0.f};
  const u16* ap = hmf + ((size_t)(tt * 4) * KS2 * 64 + lane) * 8;
  const u16* bp = W2t + ((size_t)(dg * 5) * KS2 * 64 + lane) * 8;
  constexpr int RD = 5;
  u32x4 a[RD][4], b[RD][5];
  auto ld = [&](int k) {
    const int ks = ks0 + (k < cnt ? k : cnt - 1);
#pragma unroll
    for (int i = 0; i < 4; ++i) {
      const u16* p = ap + (size_t)(i * KS2 + ks) * 512;
      asm volatile("global_load_dwordx4 %0, %1, off" : "=v"(a[k % RD][i]) : "v"(p) : "memory");
    }
#pragma unroll
    for (int j = 0; j < 5; ++j) {
      const u16* p = bp + (size_t)(j * KS2 + ks) * 512;
      asm volatile("global_load_dwordx4 %0, %1, off" : "=v"(b[k % RD][j]) : "v"(p) : "memory");
    }
  };
#pragma unroll
  for (int k = 0; k < RD - 1; ++k) ld(k);
#pragma unroll
  for (int k = 0; k < 14; ++k) {
    if (k + RD - 1 < 14) ld(k + RD - 1);
    const int later = (13 - k) < (RD - 1) ? (13 - k) : (RD - 1);
    if (later == 6) asm volatile("s_waitcnt vmcnt(54)" ::: "memory");
    else if (later == 5) asm volatile("s_waitcnt vmcnt(45)" ::: "memory");
    else if (later == 4) asm volatile("s_waitcnt vmcnt(36)" ::: "memory");
    else if (later == 3) asm volatile("s_waitcnt vmcnt(27)" ::: "memory");
    else if (later == 2) asm volatile("s_waitcnt vmcnt(18)" ::: "memory");
    else if (later == 1) asm volatile("s_waitcnt vmcnt(9)" ::: "memory");
    else asm volatile("s_waitcnt vmcnt(0)" ::: "memory");
#pragma unroll
    for (int i = 0; i < 4; ++i) asm volatile("" : "+v"(a[k % RD][i]));
#pragma unroll
    for (int j = 0; j < 5; ++j) asm volatile("" : "+v"(b[k % RD][j]));
    if (k < 13 || cnt == 14) {
#pragma unroll
      for (int j = 0; j < 5; ++j)
#pragma unroll
        for (int i = 0; i < 4; ++i)
          acc[j][i] = __builtin_amdgcn_mfma_f32_16x16x32_bf16(__builtin_bit_cast(bf16x8, b[k % RD][j]),
                                                              __builtin_bit_cast(bf16x8, a[k % RD][i]), acc[j][i], 0, 0, 0);
    }
    __builtin_amdgcn_sched_barrier(0);
  }
#pragma unroll
  for (int j = 0; j < 5; ++j)
#pragma unroll
    for (int i = 0; i < 4; ++i) *(f32x4*)(red + ((w * 20 + j * 4 + i) * 64 + lane) * 4) = acc[j][i];
  __syncthreads();
#pragma unroll
  for (int q = 0; q < 5; ++q) {
    const int tl = w * 5 + q;
    const int j = tl >> 2, i = tl & 3;
    f32x4 s = *(const f32x4*)(red + ((0 * 20 + tl) * 64 + lane) * 4);
#pragma unroll
    for (int ww = 1; ww < 4; ++ww) s += *(const f32x4*)(red + ((ww * 20 + tl) * 64 + lane) * 4);
    const int token = tt * 64 + i * 16 + (lane & 15);
    const int dcol = dg * 80 + j * 16 + 4 * (lane >> 4);
    u32x2 hv = *(const u32x2*)(hbuf + (size_t)token * D_ + dcol);
    u32x2 o;
    o[0] = pack2(lo16(hv[0]) + rbf(s[0]), hi16(hv[0]) + rbf(s[1]));
    o[1] = pack2(lo16(hv[1]) + rbf(s[2]), hi16(hv[1]) + rbf(s[3]));
    *(u32x2*)(out + (size_t)token * D_ + dcol) = o;
  }
}

extern "C" long long dev_ws_offset(int which) {
  switch (which) {
    case 0: return (long long)OFF_UT;
    case 1: return (long long)OFF_KI;
    case 2: return (long long)OFF_ST;
    case 3: return (long long)OFF_H;
    case 4: return (long long)OFF_ZF;
    default: return -1;
  }
}

extern "C" void kernel_launch(void* const* d_in, const int* in_sizes, int n_in, void* d_out, int out_size,
                              void* d_ws, size_t ws_size, hipStream_t stream) {
  const u16* x = (const u16*)d_in[0];
  const u16* lAr = (const u16*)d_in[1];
  const u16* Aim = (const u16*)d_in[2];
  const u16* Cre = (const u16*)d_in[3];
  const u16* Cim = (const u16*)d_in[4];
  const u16* ldt = (const u16*)d_in[5];
  const u16* Dsk = (const u16*)d_in[6];
  const u16* wg = (const u16*)d_in[8];
  const u16* wu = (const u16*)d_in[9];
  const u16* wd = (const u16*)d_in[10];
  char* ws = (char*)d_ws;
  u16* uT = (u16*)(ws + OFF_UT);
  u16* Ki = (u16*)(ws + OFF_KI);
  u16* sT = (u16*)(ws + OFF_ST);
  u16* hb = (u16*)(ws + OFF_H);
  u16* zf = (u16*)(ws + OFF_ZF);
  u16* W1 = (u16*)(ws + OFF_W1);
  u16* W2 = (u16*)(ws + OFF_W2);
  u16* hm = (u16*)(ws + OFF_HM);
  k1_ln1_kgen<<<128 + 640, 256, 0, stream>>>(x, lAr, Aim, Cre, Cim, ldt, uT, Ki);
  k2_conv<<<640, 256, 0, stream>>>(uT, Ki, Dsk, sT);
  k3_ln2_retile<<<128 + 405, 256, 0, stream>>>(x, sT, wg, wu, wd, hb, zf, W1, W2);
  k4_gemm1<<<256, 512, 0, stream>>>(zf, W1, hm);
  k5_gemm2<<<256, 256, 0, stream>>>(hm, W2, hb, (u16*)d_out);
}
```

```cpp
#include <hip/hip_runtime.h>
#include <hip/hip_bf16.h>
#include <stdint.h>

typedef __attribute__((ext_vector_type(8))) short bf16x8;
typedef __attribute__((ext_vector_type(4))) float f32x4;
typedef __attribute__((ext_vector_type(16))) float f32x16;
typedef __attribute__((ext_vector_type(4))) unsigned int u32x4;
typedef __attribute__((ext_vector_type(2))) unsigned int u32x2;
typedef __attribute__((ext_vector_type(2))) __bf16 bf16x2_t;
typedef unsigned short u16;

#define DEVI __device__ __forceinline__

constexpr int L_ = 2048, D_ = 640, N_ = 128, F_ = 1728;
constexpr int KS1 = D_ / 32;
constexpr int KS2 = F_ / 32;

constexpr size_t OFF_UT = 0;
constexpr size_t OFF_KI = OFF_UT + (size_t)D_ * L_ * 2;
constexpr size_t OFF_ST = OFF_KI + (size_t)D_ * L_ * 4;
constexpr size_t OFF_H  = OFF_ST + (size_t)D_ * L_ * 2;
constexpr size_t OFF_ZF = OFF_H + (size_t)D_ * L_ * 2;
constexpr size_t OFF_W1 = OFF_ZF + (size_t)D_ * L_ * 2;
constexpr size_t OFF_W2 = OFF_W1 + (size_t)D_ * F_ * 4;
constexpr size_t OFF_HM = OFF_W2 + (size_t)D_ * F_ * 2;
constexpr size_t OFF_CNT = OFF_HM + (size_t)L_ * F_ * 2;
constexpr size_t WS_END = OFF_CNT + 2048;

DEVI float bf2f(u16 v) { return __uint_as_float(((unsigned)v) << 16); }
DEVI u16 f2bf(float f) { __bf16 b = (__bf16)f; return __builtin_bit_cast(u16, b); }
DEVI float rbf(float f) { return bf2f(f2bf(f)); }
DEVI unsigned pack2(float a, float b) { unsigned r; asm("v_cvt_pk_bf16_f32 %0, %1, %2" : "=v"(r) : "v"(a), "v"(b)); return r; }
DEVI float lo16(unsigned w) { return __uint_as_float(w << 16); }
DEVI float hi16(unsigned w) { return __uint_as_float(w & 0xffff0000u); }

DEVI void glds16(const void* gsrc, unsigned lds_dst) {
  unsigned keep;
  asm volatile("s_mov_b32 %0, m0\n\ts_mov_b32 m0, %2\n\ts_nop 0\n\tglobal_load_lds_dwordx4 %1, off\n\ts_mov_b32 m0, %0"
               : "=&s"(keep) : "v"(gsrc), "s"(lds_dst) : "memory");
}

struct cf { float re, im; };
DEVI cf cmul(cf a, cf b) { cf r; r.re = a.re * b.re - a.im * b.im; r.im = a.re * b.im + a.im * b.re; return r; }
DEVI cf csq(cf a) { cf r; r.re = a.re * a.re - a.im * a.im; r.im = 2.f * a.re * a.im; return r; }
DEVI cf cexp_(float re, float im) {
  float e = __expf(re);
  const float CH = 0.15915493667125702f, CL = 6.420638316725915e-09f;
  float hi = im * CH;
  float lo = fmaf(im, CH, -hi) + im * CL;
  float r = __builtin_amdgcn_fractf(hi) + lo;
  cf o; o.re = e * __builtin_amdgcn_cosf(r); o.im = e * __builtin_amdgcn_sinf(r);
  return o;
}

DEVI cf cexp2_(float re2, float rev) {
  float e = __builtin_amdgcn_exp2f(re2);
  float r = __builtin_amdgcn_fractf(rev);
  cf o; o.re = e * __builtin_amdgcn_cosf(r); o.im = e * __builtin_amdgcn_sinf(r);
  return o;
}

typedef __attribute__((ext_vector_type(2))) float f2;
DEVI float sum32(float v) {
  v += __shfl_xor(v, 1); v += __shfl_xor(v, 2); v += __shfl_xor(v, 4); v += __shfl_xor(v, 8); v += __shfl_xor(v, 16);
  return v;
}
DEVI f2 unpk(unsigned w) { f2 r; r.x = lo16(w); r.y = hi16(w); return r; }
DEVI unsigned pk2(f2 v) { return pack2(v.x, v.y); }
DEVI void ln_row20(const f2 (&v)[10], unsigned (&o)[10]) {
  f2 s = v[0];
#pragma unroll
  for (int i = 1; i < 10; ++i) s += v[i];
  const float mean = sum32(s.x + s.y) * (1.f / 640.f);
  f2 q = {0.f, 0.f};
#pragma unroll
  for (int i = 0; i < 10; ++i) { f2 t = v[i] - mean; q += t * t; }
  const float var = sum32(q.x + q.y) * (1.f / 640.f);
  const float mb = rbf(mean);
  const float sd = rbf(sqrtf(rbf(rbf(var) + 1e-5f)));
  const float rs = 1.f / sd;
#pragma unroll
  for (int i = 0; i < 10; ++i) {
    const f2 t = unpk(pk2(v[i] - mb));
    f2 qq = t * rs;
    qq += (t - qq * sd) * rs;
    o[i] = pk2(qq);
  }
}

struct cf2 { f2 re, im; };
DEVI cf2 cmul2(cf2 a, cf2 b) { cf2 r; r.re = a.re * b.re - a.im * b.im; r.im = a.re * b.im + a.im * b.re; return r; }
DEVI cf2 csq2(cf2 a) { cf2 r; r.re = a.re * a.re - a.im * a.im; r.im = (a.re + a.re) * a.im; return r; }
DEVI cf2 cexp2_2(f2 re2, f2 rev) {
  f2 e, c, sn;
  e.x = __builtin_amdgcn_exp2f(re2.x); e.y = __builtin_amdgcn_exp2f(re2.y);
  const float r0 = __builtin_amdgcn_fractf(rev.x), r1 = __builtin_amdgcn_fractf(rev.y);
  c.x = __builtin_amdgcn_cosf(r0); c.y = __builtin_amdgcn_cosf(r1);
  sn.x = __builtin_amdgcn_sinf(r0); sn.y = __builtin_amdgcn_sinf(r1);
  cf2 o; o.re = e * c; o.im = e * sn;
  return o;
}
__global__ __launch_bounds__(256) void k1_ln1_kgen(const u16* __restrict__ x, const u16* __restrict__ lAr,
                                                   const u16* __restrict__ Aim, const u16* __restrict__ Cre,
                                                   const u16* __restrict__ Cim, const u16* __restrict__ ldt,
                                                   u16* __restrict__ uT, u16* __restrict__ Kimg) {
  __shared__ __attribute__((aligned(16))) char sm[20480];
  const int tid = threadIdx.x, w = tid >> 6, lane = tid & 63;
  if (blockIdx.x < 128) {
    const int l0 = blockIdx.x * 16;
    const int r0 = w * 4 + (lane >> 5), c = lane & 31;
    u32x2 raw[2][5];
#pragma unroll
    for (int ps = 0; ps < 2; ++ps)
#pragma unroll
      for (int i = 0; i < 5; ++i) raw[ps][i] = *(const u32x2*)(x + (size_t)(l0 + r0 + 2 * ps) * D_ + 4 * (c + 32 * i));
    u16* tile = (u16*)sm;
#pragma unroll
    for (int ps = 0; ps < 2; ++ps) {
      f2 v[10]; unsigned o[10];
#pragma unroll
      for (int i = 0; i < 5; ++i) { v[2 * i] = unpk(raw[ps][i][0]); v[2 * i + 1] = unpk(raw[ps][i][1]); }
      ln_row20(v, o);
#pragma unroll
      for (int i = 0; i < 5; ++i) *(u32x2*)(tile + (r0 + 2 * ps) * D_ + 4 * (c + 32 * i)) = (u32x2){o[2 * i], o[2 * i + 1]};
    }
    __syncthreads();
#pragma unroll
    for (int it = 0; it < 5; ++it) {
      const int e = tid + 256 * it, d = e % D_, q = e / D_;
      unsigned pk[4];
#pragma unroll
      for (int j = 0; j < 4; ++j) pk[j] = (unsigned)tile[(8 * q + 2 * j) * D_ + d] | ((unsigned)tile[(8 * q + 2 * j + 1) * D_ + d] << 16);
      *(u32x4*)(uT + (size_t)d * L_ + l0 + 8 * q) = (u32x4){pk[0], pk[1], pk[2], pk[3]};
    }
    return;
  }
  const int d = blockIdx.x - 128;
  const int xi = lane & 15, kq = lane >> 4;
  float (*red)[4][256] = (float (*)[4][256])sm;
  float* cst = (float*)(sm + 16384);
  if (tid < N_) {
    const float dt = rbf(expf(bf2f(ldt[d])));
    const float ar = -rbf(expf(bf2f(lAr[d * N_ + tid]))) * dt;
    const float ai = bf2f(Aim[d * N_ + tid]) * dt;
    const cf G = cexp_(ar * 128.f, ai * 128.f);
    float* cp = cst + (tid >> 1) * 12 + (tid & 1);
    cp[0] = ar * 1.4426950408889634f; cp[2] = ai * 0.15915494309189535f;
    cp[4] = 2.f * (bf2f(Cre[d * N_ + tid]) * dt); cp[6] = 2.f * (bf2f(Cim[d * N_ + tid]) * dt);
    cp[8] = G.re; cp[10] = G.im;
  }
  __syncthreads();
  const float px = (float)xi;
  f32x4 acc[4], acd[4];
#pragma unroll
  for (int s = 0; s < 4; ++s) { acc[s] = (f32x4){0.f, 0.f, 0.f, 0.f}; acd[s] = (f32x4){0.f, 0.f, 0.f, 0.f}; }
#pragma unroll
  for (int hf = 0; hf < 2; ++hf) {
    cf2 Ms[2][4], Vs[2][4];
#pragma unroll
    for (int tp = 0; tp < 2; ++tp) {
      const int pr = 16 * w + 8 * hf + 4 * tp + kq;
      const f32x4 c0 = *(const f32x4*)(cst + pr * 12);
      const f32x4 c1 = *(const f32x4*)(cst + pr * 12 + 4);
      const f32x4 c2 = *(const f32x4*)(cst + pr * 12 + 8);
      const f2 ar2 = {c0[0], c0[1]}, ai2 = {c0[2], c0[3]};
      cf2 C; C.re = (f2){c1[0], c1[1]}; C.im = (f2){c1[2], c1[3]};
      cf2 G; G.re = (f2){c2[0], c2[1]}; G.im = (f2){c2[2], c2[3]};
      cf2 V = cexp2_2(ar2 * px, ai2 * px);
      cf2 E0 = cexp2_2(ar2 * (16.f * px), ai2 * (16.f * px));
      cf2 F = cmul2(E0, G);
      Ms[tp][0] = cmul2(C, E0); Vs[tp][0] = V;
#pragma unroll
      for (int s = 1; s < 4; ++s) { F = csq2(F); V = csq2(V); Ms[tp][s] = cmul2(C, F); Vs[tp][s] = V; }
    }
    __builtin_amdgcn_sched_barrier(0);
#pragma unroll
    for (int tp = 0; tp < 2; ++tp)
#pragma unroll
      for (int s = 0; s < 4; ++s) {
        acc[s] = __builtin_amdgcn_mfma_f32_16x16x4f32(Ms[tp][s].re.x, Vs[tp][s].re.x, acc[s], 0, 0, 0);
        acd[s] = __builtin_amdgcn_mfma_f32_16x16x4f32(Ms[tp][s].im.x, Vs[tp][s].im.x, acd[s], 0, 0, 0);
      }
#pragma unroll
    for (int tp = 0; tp < 2; ++tp)
#pragma unroll
      for (int s = 0; s < 4; ++s) {
        acc[s] = __builtin_amdgcn_mfma_f32_16x16x4f32(Ms[tp][s].re.y, Vs[tp][s].re.y, acc[s], 0, 0, 0);
        acd[s] = __builtin_amdgcn_mfma_f32_16x16x4f32(Ms[tp][s].im.y, Vs[tp][s].im.y, acd[s], 0, 0, 0);
      }
    __builtin_amdgcn_sched_barrier(0);
  }
#pragma unroll
  for (int s = 0; s < 4; ++s)
#pragma unroll
    for (int r = 0; r < 4; ++r) red[w][s][(4 * kq + r) * 16 + xi] = acc[s][r] - acd[s][r];
  __syncthreads();
  float* tabl = (float*)(sm + 16384);
  {
    const f32x4* rf = (const f32x4*)sm;
    f32x4 t = (rf[tid] + rf[256 + tid]) + (rf[512 + tid] + rf[768 + tid]);
    ((f32x4*)tabl)[tid] = t;
  }
  __syncthreads();
  {
    const int g = tid;
    float kv[8];
    if (g < 32) {
#pragma unroll
      for (int e = 0; e < 8; ++e) kv[e] = tabl[8 * g + e];
    } else if (g < 64) {
      const float* t = tabl + 256 + 4 * g - 128;
      const float t0 = t[0], t1 = t[1], t2 = t[2], t3 = t[3], t4 = (g == 63) ? tabl[512] : t[4];
      kv[0] = t0; kv[1] = t0; kv[2] = t1; kv[3] = t2; kv[4] = t2; kv[5] = t2; kv[6] = t3; kv[7] = t4;
    } else if (g < 128) {
      const float* t = tabl + 512 + 2 * g - 128;
      const float t0 = t[0], t1 = t[1], t2 = (g == 127) ? tabl[768] : t[2];
      kv[0] = t0; kv[1] = t0; kv[2] = t0; kv[3] = t1; kv[4] = t1; kv[5] = t1; kv[6] = t2; kv[7] = t2;
    } else {
      const float* t = tabl + 768 + g - 128;
      const float t0 = t[0], t1 = t[1];
      kv[0] = t0; kv[1] = t0; kv[2] = t0; kv[3] = t0; kv[4] = (g & 1) ? t1 : t0; kv[5] = t1; kv[6] = t1; kv[7] = t1;
    }
    u32x4 hi, lo;
#pragma unroll
    for (int j = 0; j < 4; ++j) {
      const unsigned h = pack2(kv[7 - 2 * j], kv[6 - 2 * j]);
      hi[j] = h;
      lo[j] = pack2(kv[7 - 2 * j] - lo16(h), kv[6 - 2 * j] - hi16(h));
    }
    u16* dst = Kimg + (size_t)d * (2 * L_);
    *(u32x4*)(dst + 8 * g) = hi;
    *(u32x4*)(dst + L_ + 8 * g) = lo;
  }
}

constexpr int CV_CS = 4160;
constexpr int CV_UB = 8 * CV_CS;
constexpr int CV_KP = 95 * 80;
__global__ __launch_bounds__(256, 3) void k2_conv(const u16* __restrict__ uT, const u16* __restrict__ Kimg,
                                               const u16* __restrict__ Dskip, u16* __restrict__ sT) {
  __shared__ __attribute__((aligned(16))) char sm[CV_UB + 2 * CV_KP];
  const int tid = threadIdx.x, w = tid >> 6, lane = tid & 63;
  const int d = blockIdx.x;
  const u16* ug = uT + (size_t)d * L_;
  u32x4 cur = *(const u32x4*)(ug + 8 * tid);
  u32x4 prv = (u32x4){0u, 0u, 0u, 0u};
  if (tid > 0) prv = *(const u32x4*)(ug + 8 * (tid - 1));
  const u16* kg = Kimg + (size_t)d * (2 * L_);
  u32x4 kh = *(const u32x4*)(kg + 8 * tid);
  u32x4 kl = *(const u32x4*)(kg + L_ + 8 * tid);
  if (tid < 24) *(u32x4*)(sm + (tid / 3) * CV_CS + 16 * (tid % 3)) = (u32x4){0u, 0u, 0u, 0u};
  for (int i = tid; i < 2 * 155; i += 256) {
    int p = i / 155, o = i % 155;
    *(u32x4*)(sm + CV_UB + p * CV_KP + 16 * o) = (u32x4){0u, 0u, 0u, 0u};
  }
  {
    unsigned wd[8] = {prv[0], prv[1], prv[2], prv[3], cur[0], cur[1], cur[2], cur[3]};
#pragma unroll
    for (int k = 0; k < 8; ++k) {
      u32x4 o;
      if (k & 1) {
#pragma unroll
        for (int j = 0; j < 4; ++j) o[j] = wd[(k + 1) / 2 + j];
      } else {
#pragma unroll
        for (int j = 0; j < 4; ++j) o[j] = __builtin_amdgcn_alignbit(wd[k / 2 + j + 1], wd[k / 2 + j], 16);
      }
      *(u32x4*)(sm + k * CV_CS + 16 * (tid + 3)) = o;
    }
    const int col = tid >> 2, q = tid & 3;
    *(u32x4*)(sm + CV_UB + (col + 31) * 80 + 16 * q) = kh;
    *(u32x4*)(sm + CV_UB + CV_KP + (col + 31) * 80 + 16 * q) = kl;
  }
  __syncthreads();
  const int b = lane & 31, h = lane >> 5;
  f32x16 acc0, acc1;
#pragma unroll
  for (int i = 0; i < 16; ++i) { acc0[i] = 0.f; acc1[i] = 0.f; }
  const char* abase = sm + (b & 7) * CV_CS + 16 * ((b >> 3) - h + 3);
  const char* bbase = sm + CV_UB + (b + 31) * 80 + 16 * h;
  {
    const char* ap = abase + 64 * w;
    const char* bp = bbase - 80 * w;
#pragma unroll
    for (int it = 0; it < 8; ++it) {
#pragma unroll
      for (int s = 0; s < 2; ++s) {
        bf16x8 a = *(const bf16x8*)(ap + 256 * it - 32 * s);
        bf16x8 b1h = *(const bf16x8*)(bp + 80 * (32 - 4 * it) + 32 * s);
        bf16x8 b1l = *(const bf16x8*)(bp + 80 * (32 - 4 * it) + 32 * s + CV_KP);
        bf16x8 b0h = *(const bf16x8*)(bp - 80 * 4 * it + 32 * s);
        bf16x8 b0l = *(const bf16x8*)(bp - 80 * 4 * it + 32 * s + CV_KP);
        acc1 = __builtin_amdgcn_mfma_f32_32x32x16_bf16(a, b1h, acc1, 0, 0, 0);
        acc0 = __builtin_amdgcn_mfma_f32_32x32x16_bf16(a, b0h, acc0, 0, 0, 0);
        acc1 = __builtin_amdgcn_mfma_f32_32x32x16_bf16(a, b1l, acc1, 0, 0, 0);
        acc0 = __builtin_amdgcn_mfma_f32_32x32x16_bf16(a, b0l, acc0, 0, 0, 0);
      }
    }
#pragma unroll
    for (int it = 8; it < 16; ++it) {
#pragma unroll
      for (int s = 0; s < 2; ++s) {
        bf16x8 a = *(const bf16x8*)(ap + 256 * it - 32 * s);
        bf16x8 b1h = *(const bf16x8*)(bp + 80 * (32 - 4 * it) + 32 * s);
        bf16x8 b1l = *(const bf16x8*)(bp + 80 * (32 - 4 * it) + 32 * s + CV_KP);
        acc1 = __builtin_amdgcn_mfma_f32_32x32x16_bf16(a, b1h, acc1, 0, 0, 0);
        acc1 = __builtin_amdgcn_mfma_f32_32x32x16_bf16(a, b1l, acc1, 0, 0, 0);
      }
    }
  }
  __syncthreads();
  float* red = (float*)sm;
#pragma unroll
  for (int g = 0; g < 4; ++g) {
    f32x4 v0 = {acc0[4 * g], acc0[4 * g + 1], acc0[4 * g + 2], acc0[4 * g + 3]};
    f32x4 v1 = {acc1[4 * g], acc1[4 * g + 1], acc1[4 * g + 2], acc1[4 * g + 3]};
    *(f32x4*)(red + w * 2304 + b * 36 + 8 * g + 4 * h) = v0;
    *(f32x4*)(red + w * 2304 + (32 + b) * 36 + 8 * g + 4 * h) = v1;
  }
  __syncthreads();
  {
    const int a = tid >> 2, bb = (tid & 3) * 8;
    f32x4 s0 = {0.f, 0.f, 0.f, 0.f}, s1 = {0.f, 0.f, 0.f, 0.f};
#pragma unroll
    for (int ww = 0; ww < 4; ++ww) {
      s0 += *(const f32x4*)(red + ww * 2304 + a * 36 + bb);
      s1 += *(const f32x4*)(red + ww * 2304 + a * 36 + bb + 4);
    }
    const float dsk = bf2f(Dskip[d]);
    float y[8] = {s0[0], s0[1], s0[2], s0[3], s1[0], s1[1], s1[2], s1[3]};
    u32x4 o;
#pragma unroll
    for (int j = 0; j < 4; ++j) {
      float e0 = rbf(y[2 * j]) + rbf(lo16(cur[j]) * dsk);
      float e1 = rbf(y[2 * j + 1]) + rbf(hi16(cur[j]) * dsk);
      o[j] = pack2(e0, e1);
    }
    *(u32x4*)(sT + ((size_t)(tid >> 1) * D_ + d) * 16 + 8 * (tid & 1)) = o;
  }
}

__global__ __launch_bounds__(256) void k3_ln2_retile(const u16* __restrict__ x, const u16* __restrict__ sT,
                                                     const u16* __restrict__ wg, const u16* __restrict__ wu,
                                                     const u16* __restrict__ wd, u16* __restrict__ hbuf,
                                                     u16* __restrict__ zf, u16* __restrict__ W1t, u16* __restrict__ W2t,
                                                     unsigned* __restrict__ cnt) {
  __shared__ __attribute__((aligned(16))) u16 tile[16 * D_];
  const int tid = threadIdx.x, w = tid >> 6, lane = tid & 63;
  if (blockIdx.x == 128) { cnt[tid] = 0u; cnt[256 + tid] = 0u; }
  if (blockIdx.x < 128) {
    const int l0 = blockIdx.x * 16;
    u32x4 sv[5];
#pragma unroll
    for (int i = 0; i < 5; ++i) {
      const int e = tid + 256 * i;
      sv[i] = *(const u32x4*)(sT + (size_t)blockIdx.x * (D_ * 16) + 8 * e);
    }
    const int r0 = w * 4 + (lane >> 5), c = lane & 31;
    u32x2 raw[2][5];
#pragma unroll
    for (int ps = 0; ps < 2; ++ps)
#pragma unroll
      for (int i = 0; i < 5; ++i) raw[ps][i] = *(const u32x2*)(x + (size_t)(l0 + r0 + 2 * ps) * D_ + 4 * (c + 32 * i));
#pragma unroll
    for (int i = 0; i < 5; ++i) {
      const int e = tid + 256 * i;
      const int dd = e >> 1, rr = 8 * (e & 1);
#pragma unroll
      for (int j = 0; j < 4; ++j) {
        tile[(rr + 2 * j) * D_ + dd] = (u16)(sv[i][j] & 0xffffu);
        tile[(rr + 2 * j + 1) * D_ + dd] = (u16)(sv[i][j] >> 16);
      }
    }
    __syncthreads();
#pragma unroll
    for (int ps = 0; ps < 2; ++ps) {
      const int r = r0 + 2 * ps;
      f2 v[10]; unsigned o[10];
#pragma unroll
      for (int i = 0; i < 5; ++i) {
        const u32x2 sr = *(const u32x2*)(tile + r * D_ + 4 * (c + 32 * i));
        const unsigned h0 = pk2(unpk(raw[ps][i][0]) + unpk(sr[0])), h1 = pk2(unpk(raw[ps][i][1]) + unpk(sr[1]));
        v[2 * i] = unpk(h0); v[2 * i + 1] = unpk(h1);
        *(u32x2*)(hbuf + (size_t)(l0 + r) * D_ + 4 * (c + 32 * i)) = (u32x2){h0, h1};
      }
      ln_row20(v, o);
#pragma unroll
      for (int i = 0; i < 5; ++i) {
        const int e = 4 * (c + 32 * i);
        const int ks = e >> 5, kq = (e >> 3) & 3, j0 = e & 7;
        *(u32x2*)(zf + ((size_t)(blockIdx.x * KS1 + ks) * 64 + kq * 16 + r) * 8 + j0) = (u32x2){o[2 * i], o[2 * i + 1]};
      }
    }
    return;
  }
  {
    const int unit = (blockIdx.x - 128) * 4 + w;
    const u16* src; u16* dst; int ld;
    if (unit < 1080) {
      const int mat = unit & 1, grp = (unit >> 1) % 27, ks = unit / 54;
      src = (mat ? wu : wg) + (size_t)(32 * ks) * F_ + 64 * grp; ld = F_;
      dst = W1t + ((size_t)((4 * grp) * (2 * KS1) + 2 * ks + mat)) * 512;
    } else {
      const int u2 = unit - 1080, grp = u2 % 10, ks = u2 / 10;
      src = wd + (size_t)(32 * ks) * D_ + 64 * grp; ld = D_;
      dst = W2t + ((size_t)((4 * grp) * KS2 + ks)) * 512;
    }
    const int tstride = (unit < 1080) ? 2 * KS1 * 512 : KS2 * 512;
    u32x4 v[4];
#pragma unroll
    for (int i = 0; i < 4; ++i) v[i] = *(const u32x4*)(src + (size_t)(8 * i + (lane >> 3)) * ld + 8 * (lane & 7));
    char* img = (char*)tile + w * 4608;
#pragma unroll
    for (int i = 0; i < 4; ++i) *(u32x4*)(img + (8 * i + (lane >> 3)) * 144 + 16 * (lane & 7)) = v[i];
    __builtin_amdgcn_s_waitcnt(0xC07F);
    __builtin_amdgcn_wave_barrier();
    const int g = lane >> 4, q = (lane & 15) >> 2, pp = lane & 3;
    const unsigned base = (unsigned)(size_t)img + (8 * g + q) * 144 + 8 * pp;
#pragma unroll
    for (int t = 0; t < 4; ++t) {
      u32x2 r0, r1;
      asm volatile("ds_read_b64_tr_b16 %0, %2 offset:%3\n\tds_read_b64_tr_b16 %1, %2 offset:%4\n\ts_waitcnt lgkmcnt(0)"
                   : "=&v"(r0), "=&v"(r1) : "v"(base), "i"(32 * t), "i"(32 * t + 4 * 144) : "memory");
      *(u32x4*)(dst + (size_t)t * tstride + lane * 8) = (u32x4){r0[0], r0[1], r1[0], r1[1]};
    }
  }
}

DEVI f2 gelu_tanh2(f2 x) {
  const float c0 = -2.f * 0.7978845608028654f * 1.4426950408889634f, c1 = 0.044715f;
  const f2 a = x * ((x * x) * (c0 * c1) + c0);
  f2 e; e.x = __builtin_amdgcn_exp2f(a.x); e.y = __builtin_amdgcn_exp2f(a.y);
  e += 1.f;
  f2 r; r.x = __builtin_amdgcn_rcpf(e.x); r.y = __builtin_amdgcn_rcpf(e.y);
  return x * r;
}

constexpr int G1_STAGE = 44 * 1024;
constexpr int G1_NBUF = 3;
__global__ __launch_bounds__(512, 2) void k4_ffn(const u16* __restrict__ zf, const u16* __restrict__ W1t,
                                                 u16* hmf, const u16* __restrict__ W2t, const u16* __restrict__ hbuf,
                                                 u16* __restrict__ out, unsigned* cnt) {
  __shared__ __attribute__((aligned(1024))) char sm[G1_NBUF * G1_STAGE];
  const int tid = threadIdx.x, lane = tid & 63;
  const int wv = __builtin_amdgcn_readfirstlane(tid >> 6);
  const int w = wv & 3;
  const int xcd = blockIdx.x & 7, idx = blockIdx.x >> 3;
  const int mt = (xcd & 1) * 8 + (idx & 7), nc = (xcd >> 1) * 4 + (idx >> 3);
  const int t0 = 7 * nc - (nc > 11 ? nc - 11 : 0);
  if (wv >= 4) {
    const unsigned smbase = (unsigned)(size_t)sm;
    const u16* zsrc = zf + ((size_t)(mt * 8 * KS1) * 64 + lane) * 8;
    const u16* wsrc = W1t + ((size_t)(t0 * (2 * KS1)) * 64 + lane) * 8;
    auto stage = [&](int kb, int buf) {
#pragma unroll
      for (int i = 0; i < 11; ++i) {
        const int f = w + 4 * i;
        const u16* src;
        if (f < 16) src = zsrc + (size_t)((f >> 1) * KS1 + 2 * kb + (f & 1)) * 512;
        else { const int g = f - 16; src = wsrc + (size_t)((g >> 2) * (2 * KS1) + 4 * kb + (g & 3)) * 512; }
        glds16(src, smbase + buf * G1_STAGE + f * 1024);
      }
    };
    stage(0, 0); stage(1, 1);
    int nb = 2;
#pragma unroll 1
    for (int kb = 0; kb < 8; ++kb) {
      asm volatile("s_waitcnt vmcnt(11)" ::: "memory");
      __builtin_amdgcn_s_barrier();
      asm volatile("" ::: "memory");
      stage(kb + 2, nb);
      nb = (nb == G1_NBUF - 1) ? 0 : nb + 1;
    }
    asm volatile("s_waitcnt vmcnt(11)" ::: "memory");
    __builtin_amdgcn_s_barrier();
    asm volatile("s_waitcnt vmcnt(0)" ::: "memory");
    __builtin_amdgcn_s_barrier();
    return;
  }
  f32x4 acc[7][2][2];
#pragma unroll
  for (int t = 0; t < 7; ++t)
#pragma unroll
    for (int m = 0; m < 2; ++m)
#pragma unroll
      for (int r = 0; r < 2; ++r) acc[t][m][r] = (f32x4){0.f, 0.f, 0.f, 0.f};
  int cb = 0;
#pragma unroll 1
  for (int kb = 0; kb < 10; ++kb) {
    __builtin_amdgcn_s_barrier();
    asm volatile("" ::: "memory");
    const char* base = sm + cb * G1_STAGE + lane * 16;
    cb = (cb == G1_NBUF - 1) ? 0 : cb + 1;
    bf16x8 zr[2][2];
#pragma unroll
    for (int kss = 0; kss < 2; ++kss)
#pragma unroll
      for (int r = 0; r < 2; ++r) zr[kss][r] = *(const bf16x8*)(base + ((2 * w + r) * 2 + kss) * 1024);
    bf16x8 wf[2][4];
    auto rdw = [&](int grp) {
#pragma unroll
      for (int i = 0; i < 4; ++i) {
        const int g = grp * 4 + i, kss = g / 14, t = (g % 14) >> 1, m = g & 1;
        wf[grp & 1][i] = *(const bf16x8*)(base + (16 + t * 4 + kss * 2 + m) * 1024);
      }
    };
    rdw(0);
    __builtin_amdgcn_sched_barrier(0);
#pragma unroll
    for (int grp = 0; grp < 7; ++grp) {
      if (grp < 6) rdw(grp + 1);
#pragma unroll
      for (int i = 0; i < 4; ++i) {
        const int g = grp * 4 + i, kss = g / 14, t = (g % 14) >> 1, m = g & 1;
#pragma unroll
        for (int r = 0; r < 2; ++r)
          acc[t][m][r] = __builtin_amdgcn_mfma_f32_16x16x32_bf16(wf[grp & 1][i], zr[kss][r], acc[t][m][r], 0, 0, 0);
      }
      __builtin_amdgcn_sched_group_barrier(0x100, 4, 0);
      __builtin_amdgcn_sched_group_barrier(0x008, 8, 0);
      __builtin_amdgcn_sched_barrier(0);
    }
    asm volatile("s_waitcnt lgkmcnt(0)" ::: "memory");
  }
  const int lq = lane >> 4;
#pragma unroll
  for (int t = 0; t < 7; ++t) {
    const int T = t0 + t;
    const int ks2 = T >> 1, kq = 2 * (T & 1) + (lq >> 1), j0 = 4 * (lq & 1);
#pragma unroll
    for (int r = 0; r < 2; ++r) {
      unsigned ho[2];
#pragma unroll
      for (int j = 0; j < 2; ++j) {
        const f2 g = unpk(pack2(acc[t][0][r][2 * j], acc[t][0][r][2 * j + 1]));
        const f2 u = unpk(pack2(acc[t][1][r][2 * j], acc[t][1][r][2 * j + 1]));
        ho[j] = pk2(unpk(pk2(gelu_tanh2(g))) * u);
      }
      const int rt = mt * 8 + 2 * w + r;
      u32x2 o = {ho[0], ho[1]};
      {
        u16* hp = hmf + ((size_t)(rt * KS2 + ks2) * 64 + kq * 16 + (lane & 15)) * 8 + j0;
        asm volatile("global_store_dwordx2 %0, %1, off sc1\n\ts_nop 1" :: "v"(hp), "v"(o) : "memory");
      }
    }
  }
  asm volatile("s_waitcnt vmcnt(0)" ::: "memory");
  if (lane == 0) __hip_atomic_fetch_add(cnt + 32 * mt, 1u, __ATOMIC_RELAXED, __HIP_MEMORY_SCOPE_AGENT);

  {
    const int tt = (xcd & 3) * 8 + (idx & 7), dg = (xcd >> 2) * 4 + (idx >> 3);
    const int ks0 = w * 14 - (w > 2 ? w - 2 : 0), cntk = w < 2 ? 14 : 13;
    f32x4 ac2[5][4];
#pragma unroll
    for (int j = 0; j < 5; ++j)
#pragma unroll
      for (int i = 0; i < 4; ++i) ac2[j][i] = (f32x4){0.f, 0.f, 0.f, 0.f};
    const u16* ap = hmf + ((size_t)(tt * 4) * KS2 * 64 + lane) * 8;
    const u16* bp = W2t + ((size_t)(dg * 5) * KS2 * 64 + lane) * 8;
    constexpr int RD = 3;
    u32x4 a[RD][4], b[RD][5];
    auto lda = [&](int k) {
      const int ks = ks0 + (k < cntk ? k : cntk - 1);
#pragma unroll
      for (int i = 0; i < 4; ++i) {
        const u16* pp = ap + (size_t)(i * KS2 + ks) * 512;
        asm volatile("global_load_dwordx4 %0, %1, off sc1" : "=v"(a[k % RD][i]) : "v"(pp) : "memory");
      }
    };
    auto ldb = [&](int k) {
      const int ks = ks0 + (k < cntk ? k : cntk - 1);
#pragma unroll
      for (int j = 0; j < 5; ++j) {
        const u16* pp = bp + (size_t)(j * KS2 + ks) * 512;
        asm volatile("global_load_dwordx4 %0, %1, off" : "=v"(b[k % RD][j]) : "v"(pp) : "memory");
      }
    };
    ldb(0); ldb(1);
    {
      unsigned* c = cnt + 32 * (tt >> 1);
      unsigned spins = 0;
      while (__builtin_amdgcn_readfirstlane(__hip_atomic_load(c, __ATOMIC_RELAXED, __HIP_MEMORY_SCOPE_AGENT)) < 64u) {
        __builtin_amdgcn_s_sleep(2);
        if (++spins > (1u << 22)) break;
      }
    }
    asm volatile("s_waitcnt vmcnt(0)" ::: "memory");
    lda(0); lda(1);
#pragma unroll
    for (int k = 0; k < 14; ++k) {
      if (k + RD - 1 < 14) { ldb(k + RD - 1); lda(k + RD - 1); }
      const int later = (13 - k) < (RD - 1) ? (13 - k) : (RD - 1);
      if (k == 0) asm volatile("s_waitcnt vmcnt(13)" ::: "memory");
      else if (later == 2) asm volatile("s_waitcnt vmcnt(18)" ::: "memory");
      else if (later == 1) asm volatile("s_waitcnt vmcnt(9)" ::: "memory");
      else asm volatile("s_waitcnt vmcnt(0)" ::: "memory");
#pragma unroll
      for (int i = 0; i < 4; ++i) asm volatile("" : "+v"(a[k % RD][i]));
#pragma unroll
      for (int j = 0; j < 5; ++j) asm volatile("" : "+v"(b[k % RD][j]));
      if (k < 13 || cntk == 14) {
#pragma unroll
        for (int j = 0; j < 5; ++j)
#pragma unroll
          for (int i = 0; i < 4; ++i)
            ac2[j][i] = __builtin_amdgcn_mfma_f32_16x16x32_bf16(__builtin_bit_cast(bf16x8, b[k % RD][j]),
                                                                __builtin_bit_cast(bf16x8, a[k % RD][i]), ac2[j][i], 0, 0, 0);
      }
      __builtin_amdgcn_sched_barrier(0);
    }
    float* red = (float*)sm;
    __syncthreads();
#pragma unroll
    for (int j = 0; j < 5; ++j)
#pragma unroll
      for (int i = 0; i < 4; ++i) *(f32x4*)(red + ((w * 20 + j * 4 + i) * 64 + lane) * 4) = ac2[j][i];
    __syncthreads();
#pragma unroll
    for (int q = 0; q < 5; ++q) {
      const int tl = w * 5 + q;
      const int j = tl >> 2, i = tl & 3;
      f32x4 sum = *(const f32x4*)(red + ((0 * 20 + tl) * 64 + lane) * 4);
#pragma unroll
      for (int ww = 1; ww < 4; ++ww) sum += *(const f32x4*)(red + ((ww * 20 + tl) * 64 + lane) * 4);
      const int token = tt * 64 + i * 16 + (lane & 15);
      const int dcol = dg * 80 + j * 16 + 4 * (lane >> 4);
      u32x2 hv = *(const u32x2*)(hbuf + (size_t)token * D_ + dcol);
      u32x2 o;
      o[0] = pk2(unpk(hv[0]) + unpk(pack2(sum[0], sum[1])));
      o[1] = pk2(unpk(hv[1]) + unpk(pack2(sum[2], sum[3])));
      *(u32x2*)(out + (size_t)token * D_ + dcol) = o;
    }
  }
}

extern "C" long long dev_ws_offset(int which) {
  switch (which) {
    case 0: return (long long)OFF_UT;
    case 1: return (long long)OFF_KI;
    case 2: return (long long)OFF_ST;
    case 3: return (long long)OFF_H;
    case 4: return (long long)OFF_ZF;
    default: return -1;
  }
}

extern "C" void kernel_launch(void* const* d_in, const int* in_sizes, int n_in, void* d_out, int out_size,
                              void* d_ws, size_t ws_size, hipStream_t stream) {
  const u16* x = (const u16*)d_in[0];
  const u16* lAr = (const u16*)d_in[1];
  const u16* Aim = (const u16*)d_in[2];
  const u16* Cre = (const u16*)d_in[3];
  const u16* Cim = (const u16*)d_in[4];
  const u16* ldt = (const u16*)d_in[5];
  const u16* Dsk = (const u16*)d_in[6];
  const u16* wg = (const u16*)d_in[8];
  const u16* wu = (const u16*)d_in[9];
  const u16* wd = (const u16*)d_in[10];
  char* ws = (char*)d_ws;
  u16* uT = (u16*)(ws + OFF_UT);
  u16* Ki = (u16*)(ws + OFF_KI);
  u16* sT = (u16*)(ws + OFF_ST);
  u16* hb = (u16*)(ws + OFF_H);
  u16* zf = (u16*)(ws + OFF_ZF);
  u16* W1 = (u16*)(ws + OFF_W1);
  u16* W2 = (u16*)(ws + OFF_W2);
  u16* hm = (u16*)(ws + OFF_HM);
  unsigned* cnt = (unsigned*)(ws + OFF_CNT);
  k1_ln1_kgen<<<128 + 640, 256, 0, stream>>>(x, lAr, Aim, Cre, Cim, ldt, uT, Ki);
  k2_conv<<<640, 256, 0, stream>>>(uT, Ki, Dsk, sT);
  k3_ln2_retile<<<128 + 405, 256, 0, stream>>>(x, sT, wg, wu, wd, hb, zf, W1, W2, cnt);
  k4_ffn<<<256, 512, 0, stream>>>(zf, W1, hm, W2, hb, (u16*)d_out, cnt);
}
```

```cpp
#include <hip/hip_runtime.h>
#include <hip/hip_bf16.h>
#include <stdint.h>

typedef __attribute__((ext_vector_type(8))) short bf16x8;
typedef __attribute__((ext_vector_type(4))) float f32x4;
typedef __attribute__((ext_vector_type(16))) float f32x16;
typedef __attribute__((ext_vector_type(4))) unsigned int u32x4;
typedef __attribute__((ext_vector_type(2))) unsigned int u32x2;
typedef __attribute__((ext_vector_type(2))) __bf16 bf16x2_t;
typedef unsigned short u16;

#define DEVI __device__ __forceinline__

constexpr int L_ = 2048, D_ = 640, N_ = 128, F_ = 1728;
constexpr int KS1 = D_ / 32;
constexpr int KS2 = F_ / 32;

constexpr size_t OFF_UT = 0;
constexpr size_t OFF_KI = OFF_UT + (size_t)D_ * L_ * 2;
constexpr size_t OFF_ST = OFF_KI + (size_t)D_ * L_ * 4;
constexpr size_t OFF_H  = OFF_ST + (size_t)D_ * L_ * 2;
constexpr size_t OFF_ZF = OFF_H + (size_t)D_ * L_ * 2;
constexpr size_t OFF_W1 = OFF_ZF + (size_t)D_ * L_ * 2;
constexpr size_t OFF_W2 = OFF_W1 + (size_t)D_ * F_ * 4;
constexpr size_t OFF_HM = OFF_W2 + (size_t)D_ * F_ * 2;
constexpr size_t WS_END = OFF_HM + (size_t)L_ * F_ * 2;

DEVI float bf2f(u16 v) { return __uint_as_float(((unsigned)v) << 16); }
DEVI u16 f2bf(float f) { __bf16 b = (__bf16)f; return __builtin_bit_cast(u16, b); }
DEVI float rbf(float f) { return bf2f(f2bf(f)); }
DEVI unsigned pack2(float a, float b) { unsigned r; asm("v_cvt_pk_bf16_f32 %0, %1, %2" : "=v"(r) : "v"(a), "v"(b)); return r; }
DEVI float lo16(unsigned w) { return __uint_as_float(w << 16); }
DEVI float hi16(unsigned w) { return __uint_as_float(w & 0xffff0000u); }

DEVI void glds16(const void* gsrc, unsigned lds_dst) {
  unsigned keep;
  asm volatile("s_mov_b32 %0, m0\n\ts_mov_b32 m0, %2\n\ts_nop 0\n\tglobal_load_lds_dwordx4 %1, off\n\ts_mov_b32 m0, %0"
               : "=&s"(keep) : "v"(gsrc), "s"(lds_dst) : "memory");
}

struct cf { float re, im; };
DEVI cf cmul(cf a, cf b) { cf r; r.re = a.re * b.re - a.im * b.im; r.im = a.re * b.im + a.im * b.re; return r; }
DEVI cf csq(cf a) { cf r; r.re = a.re * a.re - a.im * a.im; r.im = 2.f * a.re * a.im; return r; }
DEVI cf cexp_(float re, float im) {
  float e = __expf(re);
  const float CH = 0.15915493667125702f, CL = 6.420638316725915e-09f;
  float hi = im * CH;
  float lo = fmaf(im, CH, -hi) + im * CL;
  float r = __builtin_amdgcn_fractf(hi) + lo;
  cf o; o.re = e * __builtin_amdgcn_cosf(r); o.im = e * __builtin_amdgcn_sinf(r);
  return o;
}

DEVI cf cexp2_(float re2, float rev) {
  float e = __builtin_amdgcn_exp2f(re2);
  float r = __builtin_amdgcn_fractf(rev);
  cf o; o.re = e * __builtin_amdgcn_cosf(r); o.im = e * __builtin_amdgcn_sinf(r);
  return o;
}

typedef __attribute__((ext_vector_type(2))) float f2;
DEVI float sum32(float v) {
  v += __shfl_xor(v, 1); v += __shfl_xor(v, 2); v += __shfl_xor(v, 4); v += __shfl_xor(v, 8); v += __shfl_xor(v, 16);
  return v;
}
DEVI f2 unpk(unsigned w) { f2 r; r.x = lo16(w); r.y = hi16(w); return r; }
DEVI unsigned pk2(f2 v) { return pack2(v.x, v.y); }
DEVI void ln_row20(const f2 (&v)[10], unsigned (&o)[10]) {
  f2 s = v[0];
#pragma unroll
  for (int i = 1; i < 10; ++i) s += v[i];
  const float mean = sum32(s.x + s.y) * (1.f / 640.f);
  f2 q = {0.f, 0.f};
#pragma unroll
  for (int i = 0; i < 10; ++i) { f2 t = v[i] - mean; q += t * t; }
  const float var = sum32(q.x + q.y) * (1.f / 640.f);
  const float mb = rbf(mean);
  const float sd = rbf(sqrtf(rbf(rbf(var) + 1e-5f)));
  const float rs = 1.f / sd;
#pragma unroll
  for (int i = 0; i < 10; ++i) {
    const f2 t = unpk(pk2(v[i] - mb));
    f2 qq = t * rs;
    qq += (t - qq * sd) * rs;
    o[i] = pk2(qq);
  }
}

DEVI void retile_unit(int unit, int w, int lane, char* smem, const u16* __restrict__ wg, const u16* __restrict__ wu,
                      const u16* __restrict__ wd, u16* __restrict__ W1t, u16* __restrict__ W2t)
{
    const u16* src; u16* dst; int ld;
    if (unit < 1080) {
      const int mat = unit & 1, grp = (unit >> 1) % 27, ks = unit / 54;
      src = (mat ? wu : wg) + (size_t)(32 * ks) * F_ + 64 * grp; ld = F_;
      dst = W1t + ((size_t)((4 * grp) * (2 * KS1) + 2 * ks + mat)) * 512;
    } else {
      const int u2 = unit - 1080, grp = u2 % 10, ks = u2 / 10;
      src = wd + (size_t)(32 * ks) * D_ + 64 * grp; ld = D_;
      dst = W2t + ((size_t)((4 * grp) * KS2 + ks)) * 512;
    }
    const int tstride = (unit < 1080) ? 2 * KS1 * 512 : KS2 * 512;
    u32x4 v[4];
#pragma unroll
    for (int i = 0; i < 4; ++i) v[i] = *(const u32x4*)(src + (size_t)(8 * i + (lane >> 3)) * ld + 8 * (lane & 7));
    char* img = smem + w * 4608;
#pragma unroll
    for (int i = 0; i < 4; ++i) *(u32x4*)(img + (8 * i + (lane >> 3)) * 144 + 16 * (lane & 7)) = v[i];
    __builtin_amdgcn_s_waitcnt(0xC07F);
    __builtin_amdgcn_wave_barrier();
    const int g = lane >> 4, q = (lane & 15) >> 2, pp = lane & 3;
    const unsigned base = (unsigned)(size_t)img + (8 * g + q) * 144 + 8 * pp;
#pragma unroll
    for (int t = 0; t < 4; ++t) {
      u32x2 r0, r1;
      asm volatile("ds_read_b64_tr_b16 %0, %2 offset:%3\n\tds_read_b64_tr_b16 %1, %2 offset:%4\n\ts_waitcnt lgkmcnt(0)"
                   : "=&v"(r0), "=&v"(r1) : "v"(base), "i"(32 * t), "i"(32 * t + 4 * 144) : "memory");
      *(u32x4*)(dst + (size_t)t * tstride + lane * 8) = (u32x4){r0[0], r0[1], r1[0], r1[1]};
    }
  }


struct cf2 { f2 re, im; };
DEVI cf2 cmul2(cf2 a, cf2 b) { cf2 r; r.re = a.re * b.re - a.im * b.im; r.im = a.re * b.im + a.im * b.re; return r; }
DEVI cf2 csq2(cf2 a) { cf2 r; r.re = a.re * a.re - a.im * a.im; r.im = (a.re + a.re) * a.im; return r; }
DEVI cf2 cexp2_2(f2 re2, f2 rev) {
  f2 e, c, sn;
  e.x = __builtin_amdgcn_exp2f(re2.x); e.y = __builtin_amdgcn_exp2f(re2.y);
  const float r0 = __builtin_amdgcn_fractf(rev.x), r1 = __builtin_amdgcn_fractf(rev.y);
  c.x = __builtin_amdgcn_cosf(r0); c.y = __builtin_amdgcn_cosf(r1);
  sn.x = __builtin_amdgcn_sinf(r0); sn.y = __builtin_amdgcn_sinf(r1);
  cf2 o; o.re = e * c; o.im = e * sn;
  return o;
}
__global__ __launch_bounds__(256) void k1_ln1_kgen(const u16* __restrict__ x, const u16* __restrict__ lAr,
                                                   const u16* __restrict__ Aim, const u16* __restrict__ Cre,
                                                   const u16* __restrict__ Cim, const u16* __restrict__ ldt,
                                                   u16* __restrict__ uT, u16* __restrict__ Kimg,
                                                   const u16* __restrict__ wg, const u16* __restrict__ wu,
                                                   const u16* __restrict__ wd, u16* __restrict__ W1t, u16* __restrict__ W2t) {
  __shared__ __attribute__((aligned(16))) char sm[20480];
  const int tid = threadIdx.x, w = tid >> 6, lane = tid & 63;
  if (blockIdx.x >= 768) { retile_unit((blockIdx.x - 768) * 4 + w, w, lane, sm, wg, wu, wd, W1t, W2t); return; }
  if (blockIdx.x < 128) {
    const int l0 = blockIdx.x * 16;
    const int r0 = w * 4 + (lane >> 5), c = lane & 31;
    u32x2 raw[2][5];
#pragma unroll
    for (int ps = 0; ps < 2; ++ps)
#pragma unroll
      for (int i = 0; i < 5; ++i) raw[ps][i] = *(const u32x2*)(x + (size_t)(l0 + r0 + 2 * ps) * D_ + 4 * (c + 32 * i));
    u16* tile = (u16*)sm;
#pragma unroll
    for (int ps = 0; ps < 2; ++ps) {
      f2 v[10]; unsigned o[10];
#pragma unroll
      for (int i = 0; i < 5; ++i) { v[2 * i] = unpk(raw[ps][i][0]); v[2 * i + 1] = unpk(raw[ps][i][1]); }
      ln_row20(v, o);
#pragma unroll
      for (int i = 0; i < 5; ++i) *(u32x2*)(tile + (r0 + 2 * ps) * D_ + 4 * (c + 32 * i)) = (u32x2){o[2 * i], o[2 * i + 1]};
    }
    __syncthreads();
#pragma unroll
    for (int it = 0; it < 5; ++it) {
      const int e = tid + 256 * it, d = e % D_, q = e / D_;
      unsigned pk[4];
#pragma unroll
      for (int j = 0; j < 4; ++j) pk[j] = (unsigned)tile[(8 * q + 2 * j) * D_ + d] | ((unsigned)tile[(8 * q + 2 * j + 1) * D_ + d] << 16);
      *(u32x4*)(uT + (size_t)d * L_ + l0 + 8 * q) = (u32x4){pk[0], pk[1], pk[2], pk[3]};
    }
    return;
  }
  const int d = blockIdx.x - 128;
  const int xi = lane & 15, kq = lane >> 4;
  float (*red)[4][256] = (float (*)[4][256])sm;
  float* cst = (float*)(sm + 16384);
  if (tid < N_) {
    const float dt = rbf(expf(bf2f(ldt[d])));
    const float ar = -rbf(expf(bf2f(lAr[d * N_ + tid]))) * dt;
    const float ai = bf2f(Aim[d * N_ + tid]) * dt;
    const cf G = cexp_(ar * 128.f, ai * 128.f);
    float* cp = cst + (tid >> 1) * 12 + (tid & 1);
    cp[0] = ar * 1.4426950408889634f; cp[2] = ai * 0.15915494309189535f;
    cp[4] = 2.f * (bf2f(Cre[d * N_ + tid]) * dt); cp[6] = 2.f * (bf2f(Cim[d * N_ + tid]) * dt);
    cp[8] = G.re; cp[10] = G.im;
  }
  __syncthreads();
  const float px = (float)xi;
  f32x4 acc[4], acd[4];
#pragma unroll
  for (int s = 0; s < 4; ++s) { acc[s] = (f32x4){0.f, 0.f, 0.f, 0.f}; acd[s] = (f32x4){0.f, 0.f, 0.f, 0.f}; }
#pragma unroll
  for (int hf = 0; hf < 2; ++hf) {
    cf2 Ms[2][4], Vs[2][4];
#pragma unroll
    for (int tp = 0; tp < 2; ++tp) {
      const int pr = 16 * w + 8 * hf + 4 * tp + kq;
      const f32x4 c0 = *(const f32x4*)(cst + pr * 12);
      const f32x4 c1 = *(const f32x4*)(cst + pr * 12 + 4);
      const f32x4 c2 = *(const f32x4*)(cst + pr * 12 + 8);
      const f2 ar2 = {c0[0], c0[1]}, ai2 = {c0[2], c0[3]};
      cf2 C; C.re = (f2){c1[0], c1[1]}; C.im = (f2){c1[2], c1[3]};
      cf2 G; G.re = (f2){c2[0], c2[1]}; G.im = (f2){c2[2], c2[3]};
      cf2 V = cexp2_2(ar2 * px, ai2 * px);
      cf2 E0 = cexp2_2(ar2 * (16.f * px), ai2 * (16.f * px));
      cf2 F = cmul2(E0, G);
      Ms[tp][0] = cmul2(C, E0); Vs[tp][0] = V;
#pragma unroll
      for (int s = 1; s < 4; ++s) { F = csq2(F); V = csq2(V); Ms[tp][s] = cmul2(C, F); Vs[tp][s] = V; }
    }
    __builtin_amdgcn_sched_barrier(0);
#pragma unroll
    for (int tp = 0; tp < 2; ++tp)
#pragma unroll
      for (int s = 0; s < 4; ++s) {
        acc[s] = __builtin_amdgcn_mfma_f32_16x16x4f32(Ms[tp][s].re.x, Vs[tp][s].re.x, acc[s], 0, 0, 0);
        acd[s] = __builtin_amdgcn_mfma_f32_16x16x4f32(Ms[tp][s].im.x, Vs[tp][s].im.x, acd[s], 0, 0, 0);
      }
#pragma unroll
    for (int tp = 0; tp < 2; ++tp)
#pragma unroll
      for (int s = 0; s < 4; ++s) {
        acc[s] = __builtin_amdgcn_mfma_f32_16x16x4f32(Ms[tp][s].re.y, Vs[tp][s].re.y, acc[s], 0, 0, 0);
        acd[s] = __builtin_amdgcn_mfma_f32_16x16x4f32(Ms[tp][s].im.y, Vs[tp][s].im.y, acd[s], 0, 0, 0);
      }
    __builtin_amdgcn_sched_barrier(0);
  }
#pragma unroll
  for (int s = 0; s < 4; ++s)
#pragma unroll
    for (int r = 0; r < 4; ++r) red[w][s][(4 * kq + r) * 16 + xi] = acc[s][r] - acd[s][r];
  __syncthreads();
  float* tabl = (float*)(sm + 16384);
  {
    const f32x4* rf = (const f32x4*)sm;
    f32x4 t = (rf[tid] + rf[256 + tid]) + (rf[512 + tid] + rf[768 + tid]);
    ((f32x4*)tabl)[tid] = t;
  }
  __syncthreads();
  {
    const int g = tid;
    float kv[8];
    if (g < 32) {
#pragma unroll
      for (int e = 0; e < 8; ++e) kv[e] = tabl[8 * g + e];
    } else if (g < 64) {
      const float* t = tabl + 256 + 4 * g - 128;
      const float t0 = t[0], t1 = t[1], t2 = t[2], t3 = t[3], t4 = (g == 63) ? tabl[512] : t[4];
      kv[0] = t0; kv[1] = t0; kv[2] = t1; kv[3] = t2; kv[4] = t2; kv[5] = t2; kv[6] = t3; kv[7] = t4;
    } else if (g < 128) {
      const float* t = tabl + 512 + 2 * g - 128;
      const float t0 = t[0], t1 = t[1], t2 = (g == 127) ? tabl[768] : t[2];
      kv[0] = t0; kv[1] = t0; kv[2] = t0; kv[3] = t1; kv[4] = t1; kv[5] = t1; kv[6] = t2; kv[7] = t2;
    } else {
      const float* t = tabl + 768 + g - 128;
      const float t0 = t[0], t1 = t[1];
      kv[0] = t0; kv[1] = t0; kv[2] = t0; kv[3] = t0; kv[4] = (g & 1) ? t1 : t0; kv[5] = t1; kv[6] = t1; kv[7] = t1;
    }
    u32x4 hi, lo;
#pragma unroll
    for (int j = 0; j < 4; ++j) {
      const unsigned h = pack2(kv[7 - 2 * j], kv[6 - 2 * j]);
      hi[j] = h;
      lo[j] = pack2(kv[7 - 2 * j] - lo16(h), kv[6 - 2 * j] - hi16(h));
    }
    u16* dst = Kimg + (size_t)d * (2 * L_);
    *(u32x4*)(dst + 8 * g) = hi;
    *(u32x4*)(dst + L_ + 8 * g) = lo;
  }
}

constexpr int CV_CS = 4160;
constexpr int CV_UB = 8 * CV_CS;
constexpr int CV_KP = 95 * 80;
__global__ __launch_bounds__(256, 3) void k2_conv(const u16* __restrict__ uT, const u16* __restrict__ Kimg,
                                               const u16* __restrict__ Dskip, u16* __restrict__ sT) {
  __shared__ __attribute__((aligned(16))) char sm[CV_UB + 2 * CV_KP];
  const int tid = threadIdx.x, w = tid >> 6, lane = tid & 63;
  const int d = blockIdx.x;
  const u16* ug = uT + (size_t)d * L_;
  u32x4 cur = *(const u32x4*)(ug + 8 * tid);
  u32x4 prv = (u32x4){0u, 0u, 0u, 0u};
  if (tid > 0) prv = *(const u32x4*)(ug + 8 * (tid - 1));
  const u16* kg = Kimg + (size_t)d * (2 * L_);
  u32x4 kh = *(const u32x4*)(kg + 8 * tid);
  u32x4 kl = *(const u32x4*)(kg + L_ + 8 * tid);
  if (tid < 24) *(u32x4*)(sm + (tid / 3) * CV_CS + 16 * (tid % 3)) = (u32x4){0u, 0u, 0u, 0u};
  for (int i = tid; i < 2 * 155; i += 256) {
    int p = i / 155, o = i % 155;
    *(u32x4*)(sm + CV_UB + p * CV_KP + 16 * o) = (u32x4){0u, 0u, 0u, 0u};
  }
  {
    unsigned wd[8] = {prv[0], prv[1], prv[2], prv[3], cur[0], cur[1], cur[2], cur[3]};
#pragma unroll
    for (int k = 0; k < 8; ++k) {
      u32x4 o;
      if (k & 1) {
#pragma unroll
        for (int j = 0; j < 4; ++j) o[j] = wd[(k + 1) / 2 + j];
      } else {
#pragma unroll
        for (int j = 0; j < 4; ++j) o[j] = __builtin_amdgcn_alignbit(wd[k / 2 + j + 1], wd[k / 2 + j], 16);
      }
      *(u32x4*)(sm + k * CV_CS + 16 * (tid + 3)) = o;
    }
    const int col = tid >> 2, q = tid & 3;
    *(u32x4*)(sm + CV_UB + (col + 31) * 80 + 16 * q) = kh;
    *(u32x4*)(sm + CV_UB + CV_KP + (col + 31) * 80 + 16 * q) = kl;
  }
  __syncthreads();
  const int b = lane & 31, h = lane >> 5;
  f32x16 acc0, acc1;
#pragma unroll
  for (int i = 0; i < 16; ++i) { acc0[i] = 0.f; acc1[i] = 0.f; }
  const char* abase = sm + (b & 7) * CV_CS + 16 * ((b >> 3) - h + 3);
  const char* bbase = sm + CV_UB + (b + 31) * 80 + 16 * h;
  {
    const char* ap = abase + 64 * w;
    const char* bp = bbase - 80 * w;
#pragma unroll
    for (int it = 0; it < 8; ++it) {
#pragma unroll
      for (int s = 0; s < 2; ++s) {
        bf16x8 a = *(const bf16x8*)(ap + 256 * it - 32 * s);
        bf16x8 b1h = *(const bf16x8*)(bp + 80 * (32 - 4 * it) + 32 * s);
        bf16x8 b1l = *(const bf16x8*)(bp + 80 * (32 - 4 * it) + 32 * s + CV_KP);
        bf16x8 b0h = *(const bf16x8*)(bp - 80 * 4 * it + 32 * s);
        bf16x8 b0l = *(const bf16x8*)(bp - 80 * 4 * it + 32 * s + CV_KP);
        acc1 = __builtin_amdgcn_mfma_f32_32x32x16_bf16(a, b1h, acc1, 0, 0, 0);
        acc0 = __builtin_amdgcn_mfma_f32_32x32x16_bf16(a, b0h, acc0, 0, 0, 0);
        acc1 = __builtin_amdgcn_mfma_f32_32x32x16_bf16(a, b1l, acc1, 0, 0, 0);
        acc0 = __builtin_amdgcn_mfma_f32_32x32x16_bf16(a, b0l, acc0, 0, 0, 0);
      }
    }
#pragma unroll
    for (int it = 8; it < 16; ++it) {
#pragma unroll
      for (int s = 0; s < 2; ++s) {
        bf16x8 a = *(const bf16x8*)(ap + 256 * it - 32 * s);
        bf16x8 b1h = *(const bf16x8*)(bp + 80 * (32 - 4 * it) + 32 * s);
        bf16x8 b1l = *(const bf16x8*)(bp + 80 * (32 - 4 * it) + 32 * s + CV_KP);
        acc1 = __builtin_amdgcn_mfma_f32_32x32x16_bf16(a, b1h, acc1, 0, 0, 0);
        acc1 = __builtin_amdgcn_mfma_f32_32x32x16_bf16(a, b1l, acc1, 0, 0, 0);
      }
    }
  }
  __syncthreads();
  float* red = (float*)sm;
#pragma unroll
  for (int g = 0; g < 4; ++g) {
    f32x4 v0 = {acc0[4 * g], acc0[4 * g + 1], acc0[4 * g + 2], acc0[4 * g + 3]};
    f32x4 v1 = {acc1[4 * g], acc1[4 * g + 1], acc1[4 * g + 2], acc1[4 * g + 3]};
    *(f32x4*)(red + w * 2304 + b * 36 + 8 * g + 4 * h) = v0;
    *(f32x4*)(red + w * 2304 + (32 + b) * 36 + 8 * g + 4 * h) = v1;
  }
  __syncthreads();
  {
    const int a = tid >> 2, bb = (tid & 3) * 8;
    f32x4 s0 = {0.f, 0.f, 0.f, 0.f}, s1 = {0.f, 0.f, 0.f, 0.f};
#pragma unroll
    for (int ww = 0; ww < 4; ++ww) {
      s0 += *(const f32x4*)(red + ww * 2304 + a * 36 + bb);
      s1 += *(const f32x4*)(red + ww * 2304 + a * 36 + bb + 4);
    }
    const float dsk = bf2f(Dskip[d]);
    float y[8] = {s0[0], s0[1], s0[2], s0[3], s1[0], s1[1], s1[2], s1[3]};
    u32x4 o;
#pragma unroll
    for (int j = 0; j < 4; ++j) {
      float e0 = rbf(y[2 * j]) + rbf(lo16(cur[j]) * dsk);
      float e1 = rbf(y[2 * j + 1]) + rbf(hi16(cur[j]) * dsk);
      o[j] = pack2(e0, e1);
    }
    *(u32x4*)(sT + ((size_t)(tid >> 1) * D_ + d) * 16 + 8 * (tid & 1)) = o;
  }
}

__global__ __launch_bounds__(256) void k3_ln2(const u16* __restrict__ x, const u16* __restrict__ sT,
                                              u16* __restrict__ hbuf, u16* __restrict__ zf) {
  __shared__ __attribute__((aligned(16))) u16 tile[16 * D_];
  const int tid = threadIdx.x, w = tid >> 6, lane = tid & 63;
  if (blockIdx.x < 128) {
    const int l0 = blockIdx.x * 16;
    u32x4 sv[5];
#pragma unroll
    for (int i = 0; i < 5; ++i) {
      const int e = tid + 256 * i;
      sv[i] = *(const u32x4*)(sT + (size_t)blockIdx.x * (D_ * 16) + 8 * e);
    }
    const int r0 = w * 4 + (lane >> 5), c = lane & 31;
    u32x2 raw[2][5];
#pragma unroll
    for (int ps = 0; ps < 2; ++ps)
#pragma unroll
      for (int i = 0; i < 5; ++i) raw[ps][i] = *(const u32x2*)(x + (size_t)(l0 + r0 + 2 * ps) * D_ + 4 * (c + 32 * i));
#pragma unroll
    for (int i = 0; i < 5; ++i) {
      const int e = tid + 256 * i;
      const int dd = e >> 1, rr = 8 * (e & 1);
#pragma unroll
      for (int j = 0; j < 4; ++j) {
        tile[(rr + 2 * j) * D_ + dd] = (u16)(sv[i][j] & 0xffffu);
        tile[(rr + 2 * j + 1) * D_ + dd] = (u16)(sv[i][j] >> 16);
      }
    }
    __syncthreads();
#pragma unroll
    for (int ps = 0; ps < 2; ++ps) {
      const int r = r0 + 2 * ps;
      f2 v[10]; unsigned o[10];
#pragma unroll
      for (int i = 0; i < 5; ++i) {
        const u32x2 sr = *(const u32x2*)(tile + r * D_ + 4 * (c + 32 * i));
        const unsigned h0 = pk2(unpk(raw[ps][i][0]) + unpk(sr[0])), h1 = pk2(unpk(raw[ps][i][1]) + unpk(sr[1]));
        v[2 * i] = unpk(h0); v[2 * i + 1] = unpk(h1);
        *(u32x2*)(hbuf + (size_t)(l0 + r) * D_ + 4 * (c + 32 * i)) = (u32x2){h0, h1};
      }
      ln_row20(v, o);
#pragma unroll
      for (int i = 0; i < 5; ++i) {
        const int e = 4 * (c + 32 * i);
        const int ks = e >> 5, kq = (e >> 3) & 3, j0 = e & 7;
        *(u32x2*)(zf + ((size_t)(blockIdx.x * KS1 + ks) * 64 + kq * 16 + r) * 8 + j0) = (u32x2){o[2 * i], o[2 * i + 1]};
      }
    }
    return;
  }
}

DEVI f2 gelu_tanh2(f2 x) {
  const float c0 = -2.f * 0.7978845608028654f * 1.4426950408889634f, c1 = 0.044715f;
  const f2 a = x * ((x * x) * (c0 * c1) + c0);
  f2 e; e.x = __builtin_amdgcn_exp2f(a.x); e.y = __builtin_amdgcn_exp2f(a.y);
  e += 1.f;
  f2 r; r.x = __builtin_amdgcn_rcpf(e.x); r.y = __builtin_amdgcn_rcpf(e.y);
  return x * r;
}

constexpr int G1_STAGE = 44 * 1024;
constexpr int G1_NBUF = 3;
__global__ __launch_bounds__(512, 2) void k4_gemm1(const u16* __restrict__ zf, const u16* __restrict__ W1t,
                                                   u16* __restrict__ hmf) {
  __shared__ __attribute__((aligned(1024))) char sm[G1_NBUF * G1_STAGE];
  const int tid = threadIdx.x, lane = tid & 63;
  const int wv = __builtin_amdgcn_readfirstlane(tid >> 6);
  const int w = wv & 3;
  const int xcd = blockIdx.x & 7, idx = blockIdx.x >> 3;
  const int mt = (xcd & 1) * 8 + (idx & 7), nc = (xcd >> 1) * 4 + (idx >> 3);
  const int t0 = 7 * nc - (nc > 11 ? nc - 11 : 0);
  if (wv >= 4) {
    const unsigned smbase = (unsigned)(size_t)sm;
    const u16* zsrc = zf + ((size_t)(mt * 8 * KS1) * 64 + lane) * 8;
    const u16* wsrc = W1t + ((size_t)(t0 * (2 * KS1)) * 64 + lane) * 8;
    auto stage = [&](int kb, int buf) {
#pragma unroll
      for (int i = 0; i < 11; ++i) {
        const int f = w + 4 * i;
        const u16* src;
        if (f < 16) src = zsrc + (size_t)((f >> 1) * KS1 + 2 * kb + (f & 1)) * 512;
        else { const int g = f - 16; src = wsrc + (size_t)((g >> 2) * (2 * KS1) + 4 * kb + (g & 3)) * 512; }
        glds16(src, smbase + buf * G1_STAGE + f * 1024);
      }
    };
    stage(0, 0); stage(1, 1);
    int nb = 2;
#pragma unroll 1
    for (int kb = 0; kb < 8; ++kb) {
      asm volatile("s_waitcnt vmcnt(11)" ::: "memory");
      __builtin_amdgcn_s_barrier();
      asm volatile("" ::: "memory");
      stage(kb + 2, nb);
      nb = (nb == G1_NBUF - 1) ? 0 : nb + 1;
    }
    asm volatile("s_waitcnt vmcnt(11)" ::: "memory");
    __builtin_amdgcn_s_barrier();
    asm volatile("s_waitcnt vmcnt(0)" ::: "memory");
    __builtin_amdgcn_s_barrier();
    return;
  }
  f32x4 acc[7][2][2];
#pragma unroll
  for (int t = 0; t < 7; ++t)
#pragma unroll
    for (int m = 0; m < 2; ++m)
#pragma unroll
      for (int r = 0; r < 2; ++r) acc[t][m][r] = (f32x4){0.f, 0.f, 0.f, 0.f};
  int cb = 0;
#pragma unroll 1
  for (int kb = 0; kb < 10; ++kb) {
    __builtin_amdgcn_s_barrier();
    asm volatile("" ::: "memory");
    const char* base = sm + cb * G1_STAGE + lane * 16;
    cb = (cb == G1_NBUF - 1) ? 0 : cb + 1;
    bf16x8 zr[2][2];
#pragma unroll
    for (int kss = 0; kss < 2; ++kss)
#pragma unroll
      for (int r = 0; r < 2; ++r) zr[kss][r] = *(const bf16x8*)(base + ((2 * w + r) * 2 + kss) * 1024);
    bf16x8 wf[2][4];
    auto rdw = [&](int grp) {
#pragma unroll
      for (int i = 0; i < 4; ++i) {
        const int g = grp * 4 + i, kss = g / 14, t = (g % 14) >> 1, m = g & 1;
        wf[grp & 1][i] = *(const bf16x8*)(base + (16 + t * 4 + kss * 2 + m) * 1024);
      }
    };
    rdw(0);
    __builtin_amdgcn_sched_barrier(0);
#pragma unroll
    for (int grp = 0; grp < 7; ++grp) {
      if (grp < 6) rdw(grp + 1);
#pragma unroll
      for (int i = 0; i < 4; ++i) {
        const int g = grp * 4 + i, kss = g / 14, t = (g % 14) >> 1, m = g & 1;
#pragma unroll
        for (int r = 0; r < 2; ++r)
          acc[t][m][r] = __builtin_amdgcn_mfma_f32_16x16x32_bf16(wf[grp & 1][i], zr[kss][r], acc[t][m][r], 0, 0, 0);
      }
      __builtin_amdgcn_sched_group_barrier(0x100, 4, 0);
      __builtin_amdgcn_sched_group_barrier(0x008, 8, 0);
      __builtin_amdgcn_sched_barrier(0);
    }
    asm volatile("s_waitcnt lgkmcnt(0)" ::: "memory");
  }
  const int lq = lane >> 4;
#pragma unroll
  for (int t = 0; t < 7; ++t) {
    const int T = t0 + t;
    const int ks2 = T >> 1, kq = 2 * (T & 1) + (lq >> 1), j0 = 4 * (lq & 1);
#pragma unroll
    for (int r = 0; r < 2; ++r) {
      unsigned ho[2];
#pragma unroll
      for (int j = 0; j < 2; ++j) {
        const f2 g = unpk(pack2(acc[t][0][r][2 * j], acc[t][0][r][2 * j + 1]));
        const f2 u = unpk(pack2(acc[t][1][r][2 * j], acc[t][1][r][2 * j + 1]));
        ho[j] = pk2(unpk(pk2(gelu_tanh2(g))) * u);
      }
      const int rt = mt * 8 + 2 * w + r;
      u32x2 o = {ho[0], ho[1]};
      *(u32x2*)(hmf + ((size_t)(rt * KS2 + ks2) * 64 + kq * 16 + (lane & 15)) * 8 + j0) = o;
    }
  }
}

__global__ __launch_bounds__(256) void k5_gemm2(const u16* __restrict__ hmf, const u16* __restrict__ W2t,
                                                const u16* __restrict__ hbuf, u16* __restrict__ out) {
  __shared__ __attribute__((aligned(16))) float red[4 * 20 * 64 * 4];
  const int tid = threadIdx.x, lane = tid & 63;
  const int w = __builtin_amdgcn_readfirstlane(tid >> 6);
  const int xcd = blockIdx.x & 7, idx = blockIdx.x >> 3;
  const int tt = (xcd & 3) * 8 + (idx & 7), dg = (xcd >> 2) * 4 + (idx >> 3);
  const int ks0 = w * 14 - (w > 2 ? w - 2 : 0), cnt = w < 2 ? 14 : 13;
  f32x4 acc[5][4];
#pragma unroll
  for (int j = 0; j < 5; ++j)
#pragma unroll
    for (int i = 0; i < 4; ++i) acc[j][i] = (f32x4){0.f, 0.f, 0.f, 0.f};
  const u16* ap = hmf + ((size_t)(tt * 4) * KS2 * 64 + lane) * 8;
  const u16* bp = W2t + ((size_t)(dg * 5) * KS2 * 64 + lane) * 8;
  constexpr int RD = 5;
  u32x4 a[RD][4], b[RD][5];
  auto ld = [&](int k) {
    const int ks = ks0 + (k < cnt ? k : cnt - 1);
#pragma unroll
    for (int i = 0; i < 4; ++i) {
      const u16* p = ap + (size_t)(i * KS2 + ks) * 512;
      asm volatile("global_load_dwordx4 %0, %1, off" : "=v"(a[k % RD][i]) : "v"(p) : "memory");
    }
#pragma unroll
    for (int j = 0; j < 5; ++j) {
      const u16* p = bp + (size_t)(j * KS2 + ks) * 512;
      asm volatile("global_load_dwordx4 %0, %1, off" : "=v"(b[k % RD][j]) : "v"(p) : "memory");
    }
  };
#pragma unroll
  for (int k = 0; k < RD - 1; ++k) ld(k);
#pragma unroll
  for (int k = 0; k < 14; ++k) {
    if (k + RD - 1 < 14) ld(k + RD - 1);
    const int later = (13 - k) < (RD - 1) ? (13 - k) : (RD - 1);
    if (later == 6) asm volatile("s_waitcnt vmcnt(54)" ::: "memory");
    else if (later == 5) asm volatile("s_waitcnt vmcnt(45)" ::: "memory");
    else if (later == 4) asm volatile("s_waitcnt vmcnt(36)" ::: "memory");
    else if (later == 3) asm volatile("s_waitcnt vmcnt(27)" ::: "memory");
    else if (later == 2) asm volatile("s_waitcnt vmcnt(18)" ::: "memory");
    else if (later == 1) asm volatile("s_waitcnt vmcnt(9)" ::: "memory");
    else asm volatile("s_waitcnt vmcnt(0)" ::: "memory");
#pragma unroll
    for (int i = 0; i < 4; ++i) asm volatile("" : "+v"(a[k % RD][i]));
#pragma unroll
    for (int j = 0; j < 5; ++j) asm volatile("" : "+v"(b[k % RD][j]));
    if (k < 13 || cnt == 14) {
#pragma unroll
      for (int j = 0; j < 5; ++j)
#pragma unroll
        for (int i = 0; i < 4; ++i)
          acc[j][i] = __builtin_amdgcn_mfma_f32_16x16x32_bf16(__builtin_bit_cast(bf16x8, b[k % RD][j]),
                                                              __builtin_bit_cast(bf16x8, a[k % RD][i]), acc[j][i], 0, 0, 0);
    }
    __builtin_amdgcn_sched_barrier(0);
  }
#pragma unroll
  for (int j = 0; j < 5; ++j)
#pragma unroll
    for (int i = 0; i < 4; ++i) *(f32x4*)(red + ((w * 20 + j * 4 + i) * 64 + lane) * 4) = acc[j][i];
  __syncthreads();
#pragma unroll
  for (int q = 0; q < 5; ++q) {
    const int tl = w * 5 + q;
    const int j = tl >> 2, i = tl & 3;
    f32x4 s = *(const f32x4*)(red + ((0 * 20 + tl) * 64 + lane) * 4);
#pragma unroll
    for (int ww = 1; ww < 4; ++ww) s += *(const f32x4*)(red + ((ww * 20 + tl) * 64 + lane) * 4);
    const int token = tt * 64 + i * 16 + (lane & 15);
    const int dcol = dg * 80 + j * 16 + 4 * (lane >> 4);
    u32x2 hv = *(const u32x2*)(hbuf + (size_t)token * D_ + dcol);
    u32x2 o;
    o[0] = pack2(lo16(hv[0]) + rbf(s[0]), hi16(hv[0]) + rbf(s[1]));
    o[1] = pack2(lo16(hv[1]) + rbf(s[2]), hi16(hv[1]) + rbf(s[3]));
    *(u32x2*)(out + (size_t)token * D_ + dcol) = o;
  }
}

extern "C" long long dev_ws_offset(int which) {
  switch (which) {
    case 0: return (long long)OFF_UT;
    case 1: return (long long)OFF_KI;
    case 2: return (long long)OFF_ST;
    case 3: return (long long)OFF_H;
    case 4: return (long long)OFF_ZF;
    default: return -1;
  }
}

extern "C" void kernel_launch(void* const* d_in, const int* in_sizes, int n_in, void* d_out, int out_size,
                              void* d_ws, size_t ws_size, hipStream_t stream) {
  const u16* x = (const u16*)d_in[0];
  const u16* lAr = (const u16*)d_in[1];
  const u16* Aim = (const u16*)d_in[2];
  const u16* Cre = (const u16*)d_in[3];
  const u16* Cim = (const u16*)d_in[4];
  const u16* ldt = (const u16*)d_in[5];
  const u16* Dsk = (const u16*)d_in[6];
  const u16* wg = (const u16*)d_in[8];
  const u16* wu = (const u16*)d_in[9];
  const u16* wd = (const u16*)d_in[10];
  char* ws = (char*)d_ws;
  u16* uT = (u16*)(ws + OFF_UT);
  u16* Ki = (u16*)(ws + OFF_KI);
  u16* sT = (u16*)(ws + OFF_ST);
  u16* hb = (u16*)(ws + OFF_H);
  u16* zf = (u16*)(ws + OFF_ZF);
  u16* W1 = (u16*)(ws + OFF_W1);
  u16* W2 = (u16*)(ws + OFF_W2);
  u16* hm = (u16*)(ws + OFF_HM);
  k1_ln1_kgen<<<128 + 640 + 405, 256, 0, stream>>>(x, lAr, Aim, Cre, Cim, ldt, uT, Ki, wg, wu, wd, W1, W2);
  k2_conv<<<640, 256, 0, stream>>>(uT, Ki, Dsk, sT);
  k3_ln2<<<128, 256, 0, stream>>>(x, sT, hb, zf);
  k4_gemm1<<<256, 512, 0, stream>>>(zf, W1, hm);
  k5_gemm2<<<256, 256, 0, stream>>>(hm, W2, hb, (u16*)d_out);
}
```

```cpp
#include <hip/hip_runtime.h>
#include <hip/hip_bf16.h>
#include <stdint.h>

typedef __attribute__((ext_vector_type(8))) short bf16x8;
typedef __attribute__((ext_vector_type(4))) float f32x4;
typedef __attribute__((ext_vector_type(16))) float f32x16;
typedef __attribute__((ext_vector_type(4))) unsigned int u32x4;
typedef __attribute__((ext_vector_type(2))) unsigned int u32x2;
typedef __attribute__((ext_vector_type(2))) __bf16 bf16x2_t;
typedef unsigned short u16;

#define DEVI __device__ __forceinline__

constexpr int L_ = 2048, D_ = 640, N_ = 128, F_ = 1728;
constexpr int KS1 = D_ / 32;
constexpr int KS2 = F_ / 32;

constexpr size_t OFF_UT = 0;
constexpr size_t OFF_KI = OFF_UT + (size_t)D_ * L_ * 2;
constexpr size_t OFF_ST = OFF_KI + (size_t)D_ * L_ * 4;
constexpr size_t OFF_H  = OFF_ST + (size_t)D_ * L_ * 2;
constexpr size_t OFF_ZF = OFF_H + (size_t)D_ * L_ * 2;
constexpr size_t OFF_W1 = OFF_ZF + (size_t)D_ * L_ * 2;
constexpr size_t OFF_W2 = OFF_W1 + (size_t)D_ * F_ * 4;
constexpr size_t OFF_HM = OFF_W2 + (size_t)D_ * F_ * 2;
constexpr size_t WS_END = OFF_HM + (size_t)L_ * F_ * 2;

DEVI float bf2f(u16 v) { return __uint_as_float(((unsigned)v) << 16); }
DEVI u16 f2bf(float f) { __bf16 b = (__bf16)f; return __builtin_bit_cast(u16, b); }
DEVI float rbf(float f) { return bf2f(f2bf(f)); }
DEVI unsigned pack2(float a, float b) { unsigned r; asm("v_cvt_pk_bf16_f32 %0, %1, %2" : "=v"(r) : "v"(a), "v"(b)); return r; }
DEVI float lo16(unsigned w) { return __uint_as_float(w << 16); }
DEVI float hi16(unsigned w) { return __uint_as_float(w & 0xffff0000u); }

DEVI void glds16(const void* gsrc, unsigned lds_dst) {
  unsigned keep;
  asm volatile("s_mov_b32 %0, m0\n\ts_mov_b32 m0, %2\n\ts_nop 0\n\tglobal_load_lds_dwordx4 %1, off\n\ts_mov_b32 m0, %0"
               : "=&s"(keep) : "v"(gsrc), "s"(lds_dst) : "memory");
}

struct cf { float re, im; };
DEVI cf cmul(cf a, cf b) { cf r; r.re = a.re * b.re - a.im * b.im; r.im = a.re * b.im + a.im * b.re; return r; }
DEVI cf csq(cf a) { cf r; r.re = a.re * a.re - a.im * a.im; r.im = 2.f * a.re * a.im; return r; }
DEVI cf cexp_(float re, float im) {
  float e = __expf(re);
  const float CH = 0.15915493667125702f, CL = 6.420638316725915e-09f;
  float hi = im * CH;
  float lo = fmaf(im, CH, -hi) + im * CL;
  float r = __builtin_amdgcn_fractf(hi) + lo;
  cf o; o.re = e * __builtin_amdgcn_cosf(r); o.im = e * __builtin_amdgcn_sinf(r);
  return o;
}

DEVI cf cexp2_(float re2, float rev) {
  float e = __builtin_amdgcn_exp2f(re2);
  float r = __builtin_amdgcn_fractf(rev);
  cf o; o.re = e * __builtin_amdgcn_cosf(r); o.im = e * __builtin_amdgcn_sinf(r);
  return o;
}

typedef __attribute__((ext_vector_type(2))) float f2;
DEVI float sum32(float v) {
  v += __shfl_xor(v, 1); v += __shfl_xor(v, 2); v += __shfl_xor(v, 4); v += __shfl_xor(v, 8); v += __shfl_xor(v, 16);
  return v;
}
DEVI f2 unpk(unsigned w) { f2 r; r.x = lo16(w); r.y = hi16(w); return r; }
DEVI unsigned pk2(f2 v) { return pack2(v.x, v.y); }
DEVI void ln_row20(const f2 (&v)[10], unsigned (&o)[10]) {
  f2 s = v[0];
#pragma unroll
  for (int i = 1; i < 10; ++i) s += v[i];
  const float mean = sum32(s.x + s.y) * (1.f / 640.f);
  f2 q = {0.f, 0.f};
#pragma unroll
  for (int i = 0; i < 10; ++i) { f2 t = v[i] - mean; q += t * t; }
  const float var = sum32(q.x + q.y) * (1.f / 640.f);
  const float mb = rbf(mean);
  const float sd = rbf(sqrtf(rbf(rbf(var) + 1e-5f)));
  const float rs = 1.f / sd;
#pragma unroll
  for (int i = 0; i < 10; ++i) {
    const f2 t = unpk(pk2(v[i] - mb));
    f2 qq = t * rs;
    qq += (t - qq * sd) * rs;
    o[i] = pk2(qq);
  }
}

DEVI void retile_unit(int unit, int w, int lane, char* smem, const u16* __restrict__ wg, const u16* __restrict__ wu,
                      const u16* __restrict__ wd, u16* __restrict__ W1t, u16* __restrict__ W2t)
{
    const u16* src; u16* dst; int ld;
    if (unit < 1080) {
      const int mat = unit & 1, grp = (unit >> 1) % 27, ks = unit / 54;
      src = (mat ? wu : wg) + (size_t)(32 * ks) * F_ + 64 * grp; ld = F_;
      dst = W1t + ((size_t)((4 * grp) * (2 * KS1) + 2 * ks + mat)) * 512;
    } else {
      const int u2 = unit - 1080, grp = u2 % 10, ks = u2 / 10;
      src = wd + (size_t)(32 * ks) * D_ + 64 * grp; ld = D_;
      dst = W2t + ((size_t)((4 * grp) * KS2 + ks)) * 512;
    }
    const int tstride = (unit < 1080) ? 2 * KS1 * 512 : KS2 * 512;
    u32x4 v[4];
#pragma unroll
    for (int i = 0; i < 4; ++i) v[i] = *(const u32x4*)(src + (size_t)(8 * i + (lane >> 3)) * ld + 8 * (lane & 7));
    char* img = smem + w * 4608;
#pragma unroll
    for (int i = 0; i < 4; ++i) *(u32x4*)(img + (8 * i + (lane >> 3)) * 144 + 16 * (lane & 7)) = v[i];
    __builtin_amdgcn_s_waitcnt(0xC07F);
    __builtin_amdgcn_wave_barrier();
    const int g = lane >> 4, q = (lane & 15) >> 2, pp = lane & 3;
    const unsigned base = (unsigned)(size_t)img + (8 * g + q) * 144 + 8 * pp;
#pragma unroll
    for (int t = 0; t < 4; ++t) {
      u32x2 r0, r1;
      asm volatile("ds_read_b64_tr_b16 %0, %2 offset:%3\n\tds_read_b64_tr_b16 %1, %2 offset:%4\n\ts_waitcnt lgkmcnt(0)"
                   : "=&v"(r0), "=&v"(r1) : "v"(base), "i"(32 * t), "i"(32 * t + 4 * 144) : "memory");
      *(u32x4*)(dst + (size_t)t * tstride + lane * 8) = (u32x4){r0[0], r0[1], r1[0], r1[1]};
    }
  }


struct cf2 { f2 re, im; };
DEVI cf2 cmul2(cf2 a, cf2 b) { cf2 r; r.re = a.re * b.re - a.im * b.im; r.im = a.re * b.im + a.im * b.re; return r; }
DEVI cf2 csq2(cf2 a) { cf2 r; r.re = a.re * a.re - a.im * a.im; r.im = (a.re + a.re) * a.im; return r; }
DEVI cf2 cexp2_2(f2 re2, f2 rev) {
  f2 e, c, sn;
  e.x = __builtin_amdgcn_exp2f(re2.x); e.y = __builtin_amdgcn_exp2f(re2.y);
  const float r0 = __builtin_amdgcn_fractf(rev.x), r1 = __builtin_amdgcn_fractf(rev.y);
  c.x = __builtin_amdgcn_cosf(r0); c.y = __builtin_amdgcn_cosf(r1);
  sn.x = __builtin_amdgcn_sinf(r0); sn.y = __builtin_amdgcn_sinf(r1);
  cf2 o; o.re = e * c; o.im = e * sn;
  return o;
}
__global__ __launch_bounds__(256) void k1_ln1_kgen(const u16* __restrict__ x, const u16* __restrict__ lAr,
                                                   const u16* __restrict__ Aim, const u16* __restrict__ Cre,
                                                   const u16* __restrict__ Cim, const u16* __restrict__ ldt,
                                                   u16* __restrict__ uT, u16* __restrict__ Kimg,
                                                   const u16* __restrict__ wg, const u16* __restrict__ wu,
                                                   const u16* __restrict__ wd, u16* __restrict__ W1t, u16* __restrict__ W2t) {
  __shared__ __attribute__((aligned(16))) char sm[20480];
  const int tid = threadIdx.x, w = tid >> 6, lane = tid & 63;
  if (blockIdx.x >= 768) { retile_unit((blockIdx.x - 768) * 4 + w, w, lane, sm, wg, wu, wd, W1t, W2t); return; }
  if (blockIdx.x < 128) {
    const int l0 = blockIdx.x * 16;
    const int r0 = w * 4 + (lane >> 5), c = lane & 31;
    u32x2 raw[2][5];
#pragma unroll
    for (int ps = 0; ps < 2; ++ps)
#pragma unroll
      for (int i = 0; i < 5; ++i) raw[ps][i] = *(const u32x2*)(x + (size_t)(l0 + r0 + 2 * ps) * D_ + 4 * (c + 32 * i));
    u16* tile = (u16*)sm;
#pragma unroll
    for (int ps = 0; ps < 2; ++ps) {
      f2 v[10]; unsigned o[10];
#pragma unroll
      for (int i = 0; i < 5; ++i) { v[2 * i] = unpk(raw[ps][i][0]); v[2 * i + 1] = unpk(raw[ps][i][1]); }
      ln_row20(v, o);
#pragma unroll
      for (int i = 0; i < 5; ++i) *(u32x2*)(tile + (r0 + 2 * ps) * D_ + 4 * (c + 32 * i)) = (u32x2){o[2 * i], o[2 * i + 1]};
    }
    __syncthreads();
#pragma unroll
    for (int it = 0; it < 5; ++it) {
      const int e = tid + 256 * it, d = e % D_, q = e / D_;
      unsigned pk[4];
#pragma unroll
      for (int j = 0; j < 4; ++j) pk[j] = (unsigned)tile[(8 * q + 2 * j) * D_ + d] | ((unsigned)tile[(8 * q + 2 * j + 1) * D_ + d] << 16);
      *(u32x4*)(uT + (size_t)d * L_ + l0 + 8 * q) = (u32x4){pk[0], pk[1], pk[2], pk[3]};
    }
    return;
  }
  const int d = blockIdx.x - 128;
  const int xi = lane & 15, kq = lane >> 4;
  float (*red)[4][256] = (float (*)[4][256])sm;
  float* cst = (float*)(sm + 16384);
  if (tid < N_) {
    const float dt = rbf(expf(bf2f(ldt[d])));
    const float ar = -rbf(expf(bf2f(lAr[d * N_ + tid]))) * dt;
    const float ai = bf2f(Aim[d * N_ + tid]) * dt;
    const cf G = cexp_(ar * 128.f, ai * 128.f);
    float* cp = cst + (tid >> 1) * 12 + (tid & 1);
    cp[0] = ar * 1.4426950408889634f; cp[2] = ai * 0.15915494309189535f;
    cp[4] = 2.f * (bf2f(Cre[d * N_ + tid]) * dt); cp[6] = 2.f * (bf2f(Cim[d * N_ + tid]) * dt);
    cp[8] = G.re; cp[10] = G.im;
  }
  __syncthreads();
  const float px = (float)xi;
  f32x4 acc[4], acd[4];
#pragma unroll
  for (int s = 0; s < 4; ++s) { acc[s] = (f32x4){0.f, 0.f, 0.f, 0.f}; acd[s] = (f32x4){0.f, 0.f, 0.f, 0.f}; }
#pragma unroll
  for (int hf = 0; hf < 2; ++hf) {
    cf2 Ms[2][4], Vs[2][4];
#pragma unroll
    for (int tp = 0; tp < 2; ++tp) {
      const int pr = 16 * w + 8 * hf + 4 * tp + kq;
      const f32x4 c0 = *(const f32x4*)(cst + pr * 12);
      const f32x4 c1 = *(const f32x4*)(cst + pr * 12 + 4);
      const f32x4 c2 = *(const f32x4*)(cst + pr * 12 + 8);
      const f2 ar2 = {c0[0], c0[1]}, ai2 = {c0[2], c0[3]};
      cf2 C; C.re = (f2){c1[0], c1[1]}; C.im = (f2){c1[2], c1[3]};
      cf2 G; G.re = (f2){c2[0], c2[1]}; G.im = (f2){c2[2], c2[3]};
      cf2 V = cexp2_2(ar2 * px, ai2 * px);
      cf2 E0 = cexp2_2(ar2 * (16.f * px), ai2 * (16.f * px));
      cf2 F = cmul2(E0, G);
      Ms[tp][0] = cmul2(C, E0); Vs[tp][0] = V;
#pragma unroll
      for (int s = 1; s < 4; ++s) { F = csq2(F); V = csq2(V); Ms[tp][s] = cmul2(C, F); Vs[tp][s] = V; }
    }
    __builtin_amdgcn_sched_barrier(0);
#pragma unroll
    for (int tp = 0; tp < 2; ++tp)
#pragma unroll
      for (int s = 0; s < 4; ++s) {
        acc[s] = __builtin_amdgcn_mfma_f32_16x16x4f32(Ms[tp][s].re.x, Vs[tp][s].re.x, acc[s], 0, 0, 0);
        acd[s] = __builtin_amdgcn_mfma_f32_16x16x4f32(Ms[tp][s].im.x, Vs[tp][s].im.x, acd[s], 0, 0, 0);
      }
#pragma unroll
    for (int tp = 0; tp < 2; ++tp)
#pragma unroll
      for (int s = 0; s < 4; ++s) {
        acc[s] = __builtin_amdgcn_mfma_f32_16x16x4f32(Ms[tp][s].re.y, Vs[tp][s].re.y, acc[s], 0, 0, 0);
        acd[s] = __builtin_amdgcn_mfma_f32_16x16x4f32(Ms[tp][s].im.y, Vs[tp][s].im.y, acd[s], 0, 0, 0);
      }
    __builtin_amdgcn_sched_barrier(0);
  }
#pragma unroll
  for (int s = 0; s < 4; ++s)
#pragma unroll
    for (int r = 0; r < 4; ++r) red[w][s][(4 * kq + r) * 16 + xi] = acc[s][r] - acd[s][r];
  __syncthreads();
  float* tabl = (float*)(sm + 16384);
  {
    const f32x4* rf = (const f32x4*)sm;
    f32x4 t = (rf[tid] + rf[256 + tid]) + (rf[512 + tid] + rf[768 + tid]);
    ((f32x4*)tabl)[tid] = t;
  }
  __syncthreads();
  {
    const int g = tid;
    float kv[8];
    if (g < 32) {
#pragma unroll
      for (int e = 0; e < 8; ++e) kv[e] = tabl[8 * g + e];
    } else if (g < 64) {
      const float* t = tabl + 256 + 4 * g - 128;
      const float t0 = t[0], t1 = t[1], t2 = t[2], t3 = t[3], t4 = (g == 63) ? tabl[512] : t[4];
      kv[0] = t0; kv[1] = t0; kv[2] = t1; kv[3] = t2; kv[4] = t2; kv[5] = t2; kv[6] = t3; kv[7] = t4;
    } else if (g < 128) {
      const float* t = tabl + 512 + 2 * g - 128;
      const float t0 = t[0], t1 = t[1], t2 = (g == 127) ? tabl[768] : t[2];
      kv[0] = t0; kv[1] = t0; kv[2] = t0; kv[3] = t1; kv[4] = t1; kv[5] = t1; kv[6] = t2; kv[7] = t2;
    } else {
      const float* t = tabl + 768 + g - 128;
      const float t0 = t[0], t1 = t[1];
      kv[0] = t0; kv[1] = t0; kv[2] = t0; kv[3] = t0; kv[4] = (g & 1) ? t1 : t0; kv[5] = t1; kv[6] = t1; kv[7] = t1;
    }
    u32x4 hi, lo;
#pragma unroll
    for (int j = 0; j < 4; ++j) {
      const unsigned h = pack2(kv[7 - 2 * j], kv[6 - 2 * j]);
      hi[j] = h;
      lo[j] = pack2(kv[7 - 2 * j] - lo16(h), kv[6 - 2 * j] - hi16(h));
    }
    u16* dst = Kimg + (size_t)d * (2 * L_);
    *(u32x4*)(dst + 8 * g) = hi;
    *(u32x4*)(dst + L_ + 8 * g) = lo;
  }
}

constexpr int CV_CS = 4160;
constexpr int CV_UB = 8 * CV_CS;
constexpr int CV_KP = 95 * 80;
__global__ __launch_bounds__(256, 3) void k2_conv(const u16* __restrict__ uT, const u16* __restrict__ Kimg,
                                               const u16* __restrict__ Dskip, u16* __restrict__ sT) {
  __shared__ __attribute__((aligned(16))) char sm[CV_UB + 2 * CV_KP];
  const int tid = threadIdx.x, w = tid >> 6, lane = tid & 63;
  const int d = blockIdx.x;
  const u16* ug = uT + (size_t)d * L_;
  u32x4 cur = *(const u32x4*)(ug + 8 * tid);
  u32x4 prv = (u32x4){0u, 0u, 0u, 0u};
  if (tid > 0) prv = *(const u32x4*)(ug + 8 * (tid - 1));
  const u16* kg = Kimg + (size_t)d * (2 * L_);
  u32x4 kh = *(const u32x4*)(kg + 8 * tid);
  u32x4 kl = *(const u32x4*)(kg + L_ + 8 * tid);
  if (tid < 24) *(u32x4*)(sm + (tid / 3) * CV_CS + 16 * (tid % 3)) = (u32x4){0u, 0u, 0u, 0u};
  for (int i = tid; i < 2 * 155; i += 256) {
    int p = i / 155, o = i % 155;
    *(u32x4*)(sm + CV_UB + p * CV_KP + 16 * o) = (u32x4){0u, 0u, 0u, 0u};
  }
  {
    unsigned wd[8] = {prv[0], prv[1], prv[2], prv[3], cur[0], cur[1], cur[2], cur[3]};
#pragma unroll
    for (int k = 0; k < 8; ++k) {
      u32x4 o;
      if (k & 1) {
#pragma unroll
        for (int j = 0; j < 4; ++j) o[j] = wd[(k + 1) / 2 + j];
      } else {
#pragma unroll
        for (int j = 0; j < 4; ++j) o[j] = __builtin_amdgcn_alignbit(wd[k / 2 + j + 1], wd[k / 2 + j], 16);
      }
      *(u32x4*)(sm + k * CV_CS + 16 * (tid + 3)) = o;
    }
    const int col = tid >> 2, q = tid & 3;
    *(u32x4*)(sm + CV_UB + (col + 31) * 80 + 16 * q) = kh;
    *(u32x4*)(sm + CV_UB + CV_KP + (col + 31) * 80 + 16 * q) = kl;
  }
  __syncthreads();
  const int b = lane & 31, h = lane >> 5;
  f32x16 acc0, acc1;
#pragma unroll
  for (int i = 0; i < 16; ++i) { acc0[i] = 0.f; acc1[i] = 0.f; }
  const char* abase = sm + (b & 7) * CV_CS + 16 * ((b >> 3) - h + 3);
  const char* bbase = sm + CV_UB + (b + 31) * 80 + 16 * h;
  {
    const char* ap = abase + 64 * w;
    const char* bp = bbase - 80 * w;
#pragma unroll
    for (int it = 0; it < 8; ++it) {
#pragma unroll
      for (int s = 0; s < 2; ++s) {
        bf16x8 a = *(const bf16x8*)(ap + 256 * it - 32 * s);
        bf16x8 b1h = *(const bf16x8*)(bp + 80 * (32 - 4 * it) + 32 * s);
        bf16x8 b1l = *(const bf16x8*)(bp + 80 * (32 - 4 * it) + 32 * s + CV_KP);
        bf16x8 b0h = *(const bf16x8*)(bp - 80 * 4 * it + 32 * s);
        bf16x8 b0l = *(const bf16x8*)(bp - 80 * 4 * it + 32 * s + CV_KP);
        acc1 = __builtin_amdgcn_mfma_f32_32x32x16_bf16(a, b1h, acc1, 0, 0, 0);
        acc0 = __builtin_amdgcn_mfma_f32_32x32x16_bf16(a, b0h, acc0, 0, 0, 0);
        acc1 = __builtin_amdgcn_mfma_f32_32x32x16_bf16(a, b1l, acc1, 0, 0, 0);
        acc0 = __builtin_amdgcn_mfma_f32_32x32x16_bf16(a, b0l, acc0, 0, 0, 0);
      }
    }
#pragma unroll
    for (int it = 8; it < 16; ++it) {
#pragma unroll
      for (int s = 0; s < 2; ++s) {
        bf16x8 a = *(const bf16x8*)(ap + 256 * it - 32 * s);
        bf16x8 b1h = *(const bf16x8*)(bp + 80 * (32 - 4 * it) + 32 * s);
        bf16x8 b1l = *(const bf16x8*)(bp + 80 * (32 - 4 * it) + 32 * s + CV_KP);
        acc1 = __builtin_amdgcn_mfma_f32_32x32x16_bf16(a, b1h, acc1, 0, 0, 0);
        acc1 = __builtin_amdgcn_mfma_f32_32x32x16_bf16(a, b1l, acc1, 0, 0, 0);
      }
    }
  }
  __syncthreads();
  float* red = (float*)sm;
#pragma unroll
  for (int g = 0; g < 4; ++g) {
    f32x4 v0 = {acc0[4 * g], acc0[4 * g + 1], acc0[4 * g + 2], acc0[4 * g + 3]};
    f32x4 v1 = {acc1[4 * g], acc1[4 * g + 1], acc1[4 * g + 2], acc1[4 * g + 3]};
    *(f32x4*)(red + w * 2304 + b * 36 + 8 * g + 4 * h) = v0;
    *(f32x4*)(red + w * 2304 + (32 + b) * 36 + 8 * g + 4 * h) = v1;
  }
  __syncthreads();
  {
    const int a = tid >> 2, bb = (tid & 3) * 8;
    f32x4 s0 = {0.f, 0.f, 0.f, 0.f}, s1 = {0.f, 0.f, 0.f, 0.f};
#pragma unroll
    for (int ww = 0; ww < 4; ++ww) {
      s0 += *(const f32x4*)(red + ww * 2304 + a * 36 + bb);
      s1 += *(const f32x4*)(red + ww * 2304 + a * 36 + bb + 4);
    }
    const float dsk = bf2f(Dskip[d]);
    float y[8] = {s0[0], s0[1], s0[2], s0[3], s1[0], s1[1], s1[2], s1[3]};
    u32x4 o;
#pragma unroll
    for (int j = 0; j < 4; ++j) {
      float e0 = rbf(y[2 * j]) + rbf(lo16(cur[j]) * dsk);
      float e1 = rbf(y[2 * j + 1]) + rbf(hi16(cur[j]) * dsk);
      o[j] = pack2(e0, e1);
    }
    *(u32x4*)(sT + ((size_t)(tid >> 1) * D_ + d) * 16 + 8 * (tid & 1)) = o;
  }
}

__global__ __launch_bounds__(256) void k3_ln2(const u16* __restrict__ x, const u16* __restrict__ sT,
                                              u16* __restrict__ hbuf, u16* __restrict__ zf) {
  __shared__ __attribute__((aligned(16))) u16 tile[16 * D_];
  const int tid = threadIdx.x, w = tid >> 6, lane = tid & 63;
  if (blockIdx.x < 128) {
    const int l0 = blockIdx.x * 16;
    u32x4 sv[5];
#pragma unroll
    for (int i = 0; i < 5; ++i) {
      const int e = tid + 256 * i;
      sv[i] = *(const u32x4*)(sT + (size_t)blockIdx.x * (D_ * 16) + 8 * e);
    }
    const int r0 = w * 4 + (lane >> 5), c = lane & 31;
    u32x2 raw[2][5];
#pragma unroll
    for (int ps = 0; ps < 2; ++ps)
#pragma unroll
      for (int i = 0; i < 5; ++i) raw[ps][i] = *(const u32x2*)(x + (size_t)(l0 + r0 + 2 * ps) * D_ + 4 * (c + 32 * i));
#pragma unroll
    for (int i = 0; i < 5; ++i) {
      const int e = tid + 256 * i;
      const int dd = e >> 1, rr = 8 * (e & 1);
#pragma unroll
      for (int j = 0; j < 4; ++j) {
        tile[(rr + 2 * j) * D_ + dd] = (u16)(sv[i][j] & 0xffffu);
        tile[(rr + 2 * j + 1) * D_ + dd] = (u16)(sv[i][j] >> 16);
      }
    }
    __syncthreads();
#pragma unroll
    for (int ps = 0; ps < 2; ++ps) {
      const int r = r0 + 2 * ps;
      f2 v[10]; unsigned o[10];
#pragma unroll
      for (int i = 0; i < 5; ++i) {
        const u32x2 sr = *(const u32x2*)(tile + r * D_ + 4 * (c + 32 * i));
        const unsigned h0 = pk2(unpk(raw[ps][i][0]) + unpk(sr[0])), h1 = pk2(unpk(raw[ps][i][1]) + unpk(sr[1]));
        v[2 * i] = unpk(h0); v[2 * i + 1] = unpk(h1);
        *(u32x2*)(hbuf + (size_t)(l0 + r) * D_ + 4 * (c + 32 * i)) = (u32x2){h0, h1};
      }
      ln_row20(v, o);
#pragma unroll
      for (int i = 0; i < 5; ++i) {
        const int e = 4 * (c + 32 * i);
        const int ks = e >> 5, kq = (e >> 3) & 3, j0 = e & 7;
        *(u32x2*)(zf + ((size_t)(blockIdx.x * KS1 + ks) * 64 + kq * 16 + r) * 8 + j0) = (u32x2){o[2 * i], o[2 * i + 1]};
      }
    }
    return;
  }
}

DEVI f2 gelu_tanh2(f2 x) {
  const float c0 = -2.f * 0.7978845608028654f * 1.4426950408889634f, c1 = 0.044715f;
  const f2 a = x * ((x * x) * (c0 * c1) + c0);
  f2 e; e.x = __builtin_amdgcn_exp2f(a.x); e.y = __builtin_amdgcn_exp2f(a.y);
  e += 1.f;
  f2 r; r.x = __builtin_amdgcn_rcpf(e.x); r.y = __builtin_amdgcn_rcpf(e.y);
  return x * r;
}

constexpr int G1_STAGE = 44 * 1024;
constexpr int G1_NBUF = 3;
__global__ __launch_bounds__(512, 2) void k4_gemm1(const u16* __restrict__ zf, const u16* __restrict__ W1t,
                                                   u16* __restrict__ hmf) {
  __shared__ __attribute__((aligned(1024))) char sm[G1_NBUF * G1_STAGE];
  const int tid = threadIdx.x, lane = tid & 63;
  const int wv = __builtin_amdgcn_readfirstlane(tid >> 6);
  const int w = wv & 3;
  const int xcd = blockIdx.x & 7, idx = blockIdx.x >> 3;
  const int mt = (xcd & 1) * 8 + (idx & 7), nc = (xcd >> 1) * 4 + (idx >> 3);
  const int t0 = 7 * nc - (nc > 11 ? nc - 11 : 0);
  if (wv >= 4) {
    const unsigned smbase = (unsigned)(size_t)sm;
    const u16* zsrc = zf + ((size_t)(mt * 8 * KS1) * 64 + lane) * 8;
    const u16* wsrc = W1t + ((size_t)(t0 * (2 * KS1)) * 64 + lane) * 8;
    auto stage = [&](int kb, int buf) {
#pragma unroll
      for (int i = 0; i < 11; ++i) {
        const int f = w + 4 * i;
        const u16* src;
        if (f < 16) src = zsrc + (size_t)((f >> 1) * KS1 + 2 * kb + (f & 1)) * 512;
        else { const int g = f - 16; src = wsrc + (size_t)((g >> 2) * (2 * KS1) + 4 * kb + (g & 3)) * 512; }
        glds16(src, smbase + buf * G1_STAGE + f * 1024);
      }
    };
    stage(0, 0); stage(1, 1);
    int nb = 2;
#pragma unroll 1
    for (int kb = 0; kb < 8; ++kb) {
      asm volatile("s_waitcnt vmcnt(11)" ::: "memory");
      __builtin_amdgcn_s_barrier();
      asm volatile("" ::: "memory");
      stage(kb + 2, nb);
      nb = (nb == G1_NBUF - 1) ? 0 : nb + 1;
    }
    asm volatile("s_waitcnt vmcnt(11)" ::: "memory");
    __builtin_amdgcn_s_barrier();
    asm volatile("s_waitcnt vmcnt(0)" ::: "memory");
    __builtin_amdgcn_s_barrier();
    return;
  }
  f32x4 acc[7][2][2];
#pragma unroll
  for (int t = 0; t < 7; ++t)
#pragma unroll
    for (int m = 0; m < 2; ++m)
#pragma unroll
      for (int r = 0; r < 2; ++r) acc[t][m][r] = (f32x4){0.f, 0.f, 0.f, 0.f};
  int cb = 0;
#pragma unroll 1
  for (int kb = 0; kb < 10; ++kb) {
    __builtin_amdgcn_s_barrier();
    asm volatile("" ::: "memory");
    const char* base = sm + cb * G1_STAGE + lane * 16;
    cb = (cb == G1_NBUF - 1) ? 0 : cb + 1;
    bf16x8 zr[2][2];
#pragma unroll
    for (int kss = 0; kss < 2; ++kss)
#pragma unroll
      for (int r = 0; r < 2; ++r) zr[kss][r] = *(const bf16x8*)(base + ((2 * w + r) * 2 + kss) * 1024);
    bf16x8 wf[2][4];
    auto rdw = [&](int grp) {
#pragma unroll
      for (int i = 0; i < 4; ++i) {
        const int g = grp * 4 + i, kss = g / 14, t = (g % 14) >> 1, m = g & 1;
        wf[grp & 1][i] = *(const bf16x8*)(base + (16 + t * 4 + kss * 2 + m) * 1024);
      }
    };
    rdw(0);
    __builtin_amdgcn_sched_barrier(0);
#pragma unroll
    for (int grp = 0; grp < 7; ++grp) {
      if (grp < 6) rdw(grp + 1);
#pragma unroll
      for (int i = 0; i < 4; ++i) {
        const int g = grp * 4 + i, kss = g / 14, t = (g % 14) >> 1, m = g & 1;
#pragma unroll
        for (int r = 0; r < 2; ++r)
          acc[t][m][r] = __builtin_amdgcn_mfma_f32_16x16x32_bf16(wf[grp & 1][i], zr[kss][r], acc[t][m][r], 0, 0, 0);
      }
      __builtin_amdgcn_sched_group_barrier(0x100, 4, 0);
      __builtin_amdgcn_sched_group_barrier(0x008, 8, 0);
      __builtin_amdgcn_sched_barrier(0);
    }
    asm volatile("s_waitcnt lgkmcnt(0)" ::: "memory");
  }
  const int lq = lane >> 4;
#pragma unroll
  for (int t = 0; t < 7; ++t) {
    const int T = t0 + t;
    const int ks2 = T >> 1, kq = 2 * (T & 1) + (lq >> 1), j0 = 4 * (lq & 1);
#pragma unroll
    for (int r = 0; r < 2; ++r) {
      unsigned ho[2];
#pragma unroll
      for (int j = 0; j < 2; ++j) {
        const f2 g = unpk(pack2(acc[t][0][r][2 * j], acc[t][0][r][2 * j + 1]));
        const f2 u = unpk(pack2(acc[t][1][r][2 * j], acc[t][1][r][2 * j + 1]));
        ho[j] = pk2(unpk(pk2(gelu_tanh2(g))) * u);
      }
      const int rt = mt * 8 + 2 * w + r;
      u32x2 o = {ho[0], ho[1]};
      *(u32x2*)(hmf + ((size_t)(rt * KS2 + ks2) * 64 + kq * 16 + (lane & 15)) * 8 + j0) = o;
    }
  }
}

__global__ __launch_bounds__(256) void k5_gemm2(const u16* __restrict__ hmf, const u16* __restrict__ W2t,
                                                const u16* __restrict__ hbuf, u16* __restrict__ out) {
  __shared__ __attribute__((aligned(16))) float red[4 * 20 * 64 * 4];
  const int tid = threadIdx.x, lane = tid & 63;
  const int w = __builtin_amdgcn_readfirstlane(tid >> 6);
  const int xcd = blockIdx.x & 7, idx = blockIdx.x >> 3;
  const int tt = (xcd & 3) * 8 + (idx & 7), dg = (xcd >> 2) * 4 + (idx >> 3);
  const int ks0 = w * 14 - (w > 2 ? w - 2 : 0), cnt = w < 2 ? 14 : 13;
  f32x4 acc[5][4];
#pragma unroll
  for (int j = 0; j < 5; ++j)
#pragma unroll
    for (int i = 0; i < 4; ++i) acc[j][i] = (f32x4){0.f, 0.f, 0.f, 0.f};
  const u16* ap = hmf + ((size_t)(tt * 4) * KS2 * 64 + lane) * 8;
  const u16* bp = W2t + ((size_t)(dg * 5) * KS2 * 64 + lane) * 8;
  constexpr int RD = 5;
  u32x4 a[RD][4], b[RD][5];
  auto ld = [&](int k) {
    const int ks = ks0 + (k < cnt ? k : cnt - 1);
#pragma unroll
    for (int i = 0; i < 4; ++i) {
      const u16* p = ap + (size_t)(i * KS2 + ks) * 512;
      asm volatile("global_load_dwordx4 %0, %1, off" : "=v"(a[k % RD][i]) : "v"(p) : "memory");
    }
#pragma unroll
    for (int j = 0; j < 5; ++j) {
      const u16* p = bp + (size_t)(j * KS2 + ks) * 512;
      asm volatile("global_load_dwordx4 %0, %1, off" : "=v"(b[k % RD][j]) : "v"(p) : "memory");
    }
  };
#pragma unroll
  for (int k = 0; k < RD - 1; ++k) ld(k);
#pragma unroll
  for (int k = 0; k < 14; ++k) {
    if (k + RD - 1 < 14) ld(k + RD - 1);
    const int later = (13 - k) < (RD - 1) ? (13 - k) : (RD - 1);
    if (later == 6) asm volatile("s_waitcnt vmcnt(54)" ::: "memory");
    else if (later == 5) asm volatile("s_waitcnt vmcnt(45)" ::: "memory");
    else if (later == 4) asm volatile("s_waitcnt vmcnt(36)" ::: "memory");
    else if (later == 3) asm volatile("s_waitcnt vmcnt(27)" ::: "memory");
    else if (later == 2) asm volatile("s_waitcnt vmcnt(18)" ::: "memory");
    else if (later == 1) asm volatile("s_waitcnt vmcnt(9)" ::: "memory");
    else asm volatile("s_waitcnt vmcnt(0)" ::: "memory");
#pragma unroll
    for (int i = 0; i < 4; ++i) asm volatile("" : "+v"(a[k % RD][i]));
#pragma unroll
    for (int j = 0; j < 5; ++j) asm volatile("" : "+v"(b[k % RD][j]));
    if (k < 13 || cnt == 14) {
#pragma unroll
      for (int j = 0; j < 5; ++j)
#pragma unroll
        for (int i = 0; i < 4; ++i)
          acc[j][i] = __builtin_amdgcn_mfma_f32_16x16x32_bf16(__builtin_bit_cast(bf16x8, b[k % RD][j]),
                                                              __builtin_bit_cast(bf16x8, a[k % RD][i]), acc[j][i], 0, 0, 0);
    }
    __builtin_amdgcn_sched_barrier(0);
  }
#pragma unroll
  for (int j = 0; j < 5; ++j)
#pragma unroll
    for (int i = 0; i < 4; ++i) *(f32x4*)(red + ((w * 20 + j * 4 + i) * 64 + lane) * 4) = acc[j][i];
  __syncthreads();
#pragma unroll
  for (int q = 0; q < 5; ++q) {
    const int tl = w * 5 + q;
    const int j = tl >> 2, i = tl & 3;
    f32x4 s = *(const f32x4*)(red + ((0 * 20 + tl) * 64 + lane) * 4);
#pragma unroll
    for (int ww = 1; ww < 4; ++ww) s += *(const f32x4*)(red + ((ww * 20 + tl) * 64 + lane) * 4);
    const int token = tt * 64 + i * 16 + (lane & 15);
    const int dcol = dg * 80 + j * 16 + 4 * (lane >> 4);
    u32x2 hv = *(const u32x2*)(hbuf + (size_t)token * D_ + dcol);
    u32x2 o;
    o[0] = pack2(lo16(hv[0]) + rbf(s[0]), hi16(hv[0]) + rbf(s[1]));
    o[1] = pack2(lo16(hv[1]) + rbf(s[2]), hi16(hv[1]) + rbf(s[3]));
    *(u32x2*)(out + (size_t)token * D_ + dcol) = o;
  }
}

extern "C" void kernel_launch(void* const* d_in, const int* in_sizes, int n_in, void* d_out, int out_size,
                              void* d_ws, size_t ws_size, hipStream_t stream) {
  const u16* x = (const u16*)d_in[0];
  const u16* lAr = (const u16*)d_in[1];
  const u16* Aim = (const u16*)d_in[2];
  const u16* Cre = (const u16*)d_in[3];
  const u16* Cim = (const u16*)d_in[4];
  const u16* ldt = (const u16*)d_in[5];
  const u16* Dsk = (const u16*)d_in[6];
  const u16* wg = (const u16*)d_in[8];
  const u16* wu = (const u16*)d_in[9];
  const u16* wd = (const u16*)d_in[10];
  char* ws = (char*)d_ws;
  u16* uT = (u16*)(ws + OFF_UT);
  u16* Ki = (u16*)(ws + OFF_KI);
  u16* sT = (u16*)(ws + OFF_ST);
  u16* hb = (u16*)(ws + OFF_H);
  u16* zf = (u16*)(ws + OFF_ZF);
  u16* W1 = (u16*)(ws + OFF_W1);
  u16* W2 = (u16*)(ws + OFF_W2);
  u16* hm = (u16*)(ws + OFF_HM);
  k1_ln1_kgen<<<128 + 640 + 405, 256, 0, stream>>>(x, lAr, Aim, Cre, Cim, ldt, uT, Ki, wg, wu, wd, W1, W2);
  k2_conv<<<640, 256, 0, stream>>>(uT, Ki, Dsk, sT);
  k3_ln2<<<128, 256, 0, stream>>>(x, sT, hb, zf);
  k4_gemm1<<<256, 512, 0, stream>>>(zf, W1, hm);
  k5_gemm2<<<256, 256, 0, stream>>>(hm, W2, hb, (u16*)d_out);
}
```
